```python
import torch

def _softmax(x, axis=-1):
    return torch.softmax(x, dim=axis)
def _gelu(x):
    return torch.nn.functional.gelu(x, approximate="tanh")
def _silu(x):
    return torch.nn.functional.silu(x)
def _sigmoid(x):
    return torch.sigmoid(x)
def _layernorm(x, axis=-1, eps=1e-5):
    m = x.mean(dim=axis, keepdim=True)
    v = x.var(dim=axis, keepdim=True, unbiased=False)
    return (x - m) / torch.sqrt(v + eps)
def _scatter_add(tgt, idx, src, axis=0):
    return tgt.clone().index_add_(axis, idx, src)
def _segment_sum(src, seg, n):
    out = torch.zeros((n,) + src.shape[1:], dtype=src.dtype)
    return out.index_add_(0, seg, src)
def _pad(x, pw):
    flat = tuple(v for pair in reversed(pw) for v in pair)
    return torch.nn.functional.pad(x, flat)

kernel_name = 'sequential_preLN__sliding_block_cached__glu_gated_swiglu__decode'

def setup_inputs():

    _w = (lambda a: (lambda t: t.to(torch.bfloat16) if t.is_floating_point() else t)(torch.as_tensor(a)))
    def _A_setup():
        B, Sq, D = 2, 1, 576
        Hq, Hkv, Sk, Dh = 32, 16, 8192, 128
        assert Hq % Hkv == 0, (Hq, Hkv)
        return {
            "x": _w((lambda *s: torch.randn(*s, dtype=torch.bfloat16) * 0.05)(B, Sq, D)),
            "k_cache": _w((lambda *s: torch.randn(*s, dtype=torch.bfloat16) * 1.6)(B, Hkv, Sk, Dh)),
            "v_cache": _w((lambda *s: torch.randn(*s, dtype=torch.bfloat16) * 0.8)(B, Hkv, Sk, Dh)),
            "wq": _w((lambda *s: torch.randn(*s, dtype=torch.bfloat16) * 1.6)(D, Hq * Dh) * (D ** -0.5)),
            "wo": _w((lambda *s: torch.randn(*s, dtype=torch.bfloat16) * 0.8)(Hq * Dh, D) * ((Hq * Dh) ** -0.5)),
        }
    _A_in = _A_setup()

    def _B_setup():
        B, S, D, F = 2, 1, 576, 4608
        return {
            "x": _w((lambda *s: torch.randn(*s, dtype=torch.bfloat16) * 0.05)(B, S, D)),
            "w1": _w((lambda *s: torch.randn(*s, dtype=torch.bfloat16) * 0.4)(D, F) * (2.0 / D) ** 0.5),
            "w3": _w((lambda *s: torch.randn(*s, dtype=torch.bfloat16) * 0.4)(D, F) * (2.0 / D) ** 0.5),
            "w2": _w((lambda *s: torch.randn(*s, dtype=torch.bfloat16) * 0.4)(F, D) * (2.0 / F) ** 0.5),
        }
    _B_in = _B_setup()

    return {
        "A_x": _A_in["x"],
        "A_k_cache": _A_in["k_cache"],
        "A_v_cache": _A_in["v_cache"],
        "A_wq": _A_in["wq"],
        "A_wo": _A_in["wo"],
        "B_x": _B_in["x"],
        "B_w1": _B_in["w1"],
        "B_w3": _B_in["w3"],
        "B_w2": _B_in["w2"],
    }

def reference(A_x, A_k_cache, A_v_cache, A_wq, A_wo, B_x, B_w1, B_w3, B_w2):
    def _A_ref(x, k_cache, v_cache, wq, wo):
        B, Sq, D = x.shape
        Hq, Dh = 32, 128
        Hkv, Sk = k_cache.shape[1], k_cache.shape[2]
        W = min(4096, max(256, Sk // 4))
        rep = Hq // Hkv
        q = (lambda x, *d: x.permute(*d))((x @ wq).reshape(B, Sq, Hq, Dh), 0, 2, 1, 3)
        k = (lambda x, n, axis: torch.repeat_interleave(x, n, dim=axis))(k_cache[:, :, Sk - W:, :], rep, axis=1)
        v = (lambda x, n, axis: torch.repeat_interleave(x, n, dim=axis))(v_cache[:, :, Sk - W:, :], rep, axis=1)
        scores = torch.einsum("bhqd,bhkd->bhqk", q, k) * (Dh ** -0.5)
        p = _softmax(scores, axis=-1)
        o = torch.einsum("bhqk,bhkd->bhqd", p, v)
        return (lambda x, *d: x.permute(*d))(o, 0, 2, 1, 3).reshape(B, Sq, Hq * Dh) @ wo

    def _B_ref(x, w1, w3, w2):
        gate = torch.einsum("bsd,df->bsf", x, w1)
        up = torch.einsum("bsd,df->bsf", x, w3)
        h = _silu(gate) * up
        out = torch.einsum("bsf,fd->bsd", h, w2)
        return out

    x = A_x
    h = x + _A_ref(_layernorm(x, axis=-1), A_k_cache, A_v_cache, A_wq, A_wo)
    h2 = h + _B_ref(_layernorm(h, axis=-1), B_w1, B_w3, B_w2)
    return h2
```

```cpp
#include <hip/hip_runtime.h>
#include <hip/hip_bf16.h>
#include <stdint.h>

typedef unsigned short u16;
typedef unsigned v4u __attribute__((ext_vector_type(4)));
__device__ __forceinline__ float bf2f(u16 b) { return __uint_as_float(((unsigned)b) << 16); }
__device__ __forceinline__ float bflo(unsigned u) { return __uint_as_float(u << 16); }
__device__ __forceinline__ float bfhi(unsigned u) { return __uint_as_float(u & 0xffff0000u); }
__device__ __forceinline__ float rbf(float f) { return __bfloat162float(__float2bfloat16(f)); }

constexpr int B = 2, D = 576, HQ = 32, HKV = 16, SK = 8192, DH = 128, W = 2048, F = 4608;
constexpr int NS = 8;

__device__ float block_sum(float v, float* red) {
  for (int o = 32; o >= 1; o >>= 1) v += __shfl_xor(v, o);
  int w = threadIdx.x >> 6;
  __syncthreads();
  if ((threadIdx.x & 63) == 0) red[w] = v;
  __syncthreads();
  float s = 0;
  for (int i = 0; i < (int)(blockDim.x >> 6); ++i) s += red[i];
  return s;
}

__device__ void ln_row(const float* src  , float* dst, float* red) {
  float s = 0;
  for (int i = threadIdx.x; i < D; i += blockDim.x) s += src[i];
  float m = block_sum(s, red) * (1.f / D);
  float q = 0;
  for (int i = threadIdx.x; i < D; i += blockDim.x) { float d = src[i] - m; q += d * d; }
  float v = block_sum(q, red) * (1.f / D);
  float r = 1.f / sqrtf(v + 1e-5f);
  for (int i = threadIdx.x; i < D; i += blockDim.x) dst[i] = rbf((src[i] - m) * r);
  __syncthreads();
}

__global__ void k_q(const u16* x, const u16* wq, float* q) {
  __shared__ float xs[B][D], xn[B][D], red[8], part[4][B][64];
  for (int i = threadIdx.x; i < B * D; i += 256) xs[i / D][i % D] = bf2f(x[i]);
  __syncthreads();
  ln_row(xs[0], xn[0], red); ln_row(xs[1], xn[1], red);
  int w = threadIdx.x >> 6, l = threadIdx.x & 63, c = blockIdx.x * 64 + l;
  float a0 = 0, a1 = 0;
  for (int k = w * 144; k < w * 144 + 144; ++k) { float wv = bf2f(wq[(size_t)k * 4096 + c]); a0 += xn[0][k] * wv; a1 += xn[1][k] * wv; }
  part[w][0][l] = a0; part[w][1][l] = a1;
  __syncthreads();
  if (threadIdx.x < 128) { int b = threadIdx.x >> 6; float s = part[0][b][l] + part[1][b][l] + part[2][b][l] + part[3][b][l]; q[b * 4096 + c] = rbf(s); }
}

__global__ void k_attn(const float* q, const u16* kc, const u16* vc, float* po, float* pml) {
  __shared__ float qs[2][DH], p[2][256], red[8];
  int s = blockIdx.x, h = blockIdx.y, b = blockIdx.z, t = threadIdx.x;
  qs[t >> 7][t & 127] = q[b * 4096 + h * 256 + t];
  __syncthreads();
  size_t base = ((size_t)(b * HKV + h) * SK + (SK - W) + s * 256) * DH;
  const v4u* kr = (const v4u*)(kc + base + (size_t)t * DH);
  float s0 = 0, s1 = 0;
  for (int i = 0; i < 16; ++i) { v4u v = kr[i];
    for (int j = 0; j < 4; ++j) { float lo = bflo(v[j]), hi = bfhi(v[j]); int d = i * 8 + j * 2;
      s0 += lo * qs[0][d] + hi * qs[0][d + 1]; s1 += lo * qs[1][d] + hi * qs[1][d + 1]; } }
  s0 *= 0.08838834764831845f; s1 *= 0.08838834764831845f;
  float m0 = s0, m1 = s1;
  for (int o = 32; o >= 1; o >>= 1) { m0 = fmaxf(m0, __shfl_xor(m0, o)); m1 = fmaxf(m1, __shfl_xor(m1, o)); }
  __shared__ float mx[2][4];
  if ((t & 63) == 0) { mx[0][t >> 6] = m0; mx[1][t >> 6] = m1; }
  __syncthreads();
  m0 = fmaxf(fmaxf(mx[0][0], mx[0][1]), fmaxf(mx[0][2], mx[0][3]));
  m1 = fmaxf(fmaxf(mx[1][0], mx[1][1]), fmaxf(mx[1][2], mx[1][3]));
  float p0 = __expf(s0 - m0), p1 = __expf(s1 - m1);
  p[0][t] = p0; p[1][t] = p1;
  float l0 = block_sum(p0, red), l1 = block_sum(p1, red);
  __syncthreads();
  int hh = t >> 7, d = t & 127;
  float acc = 0;
  for (int k = 0; k < 256; ++k) acc += p[hh][k] * bf2f(vc[base + (size_t)k * DH + d]);
  int row = (b * HQ + h * 2 + hh);
  po[((size_t)row * NS + s) * DH + d] = acc;
  if (t == 0) { pml[((b * HQ + h * 2) * NS + s) * 2] = m0; pml[((b * HQ + h * 2) * NS + s) * 2 + 1] = l0;
                pml[((b * HQ + h * 2 + 1) * NS + s) * 2] = m1; pml[((b * HQ + h * 2 + 1) * NS + s) * 2 + 1] = l1; }
}

__global__ void k_wo(const float* po, const float* pml, const u16* wo, float* ph) {
  __shared__ float o[B][DH];
  int hq = blockIdx.x, t = threadIdx.x;
  if (t < 256) { int b = t >> 7, d = t & 127, row = b * HQ + hq;
    float M = -INFINITY; for (int s = 0; s < NS; ++s) M = fmaxf(M, pml[(row * NS + s) * 2]);
    float L = 0, a = 0; for (int s = 0; s < NS; ++s) { float w = __expf(pml[(row * NS + s) * 2] - M); L += w * pml[(row * NS + s) * 2 + 1]; a += w * po[((size_t)row * NS + s) * DH + d]; }
    o[b][d] = rbf(a / L); }
  __syncthreads();
  for (int c = t; c < D; c += blockDim.x) { float a0 = 0, a1 = 0;
    for (int d = 0; d < DH; ++d) { float wv = bf2f(wo[(size_t)(hq * DH + d) * D + c]); a0 += o[0][d] * wv; a1 += o[1][d] * wv; }
    ph[(hq * B + 0) * D + c] = a0; ph[(hq * B + 1) * D + c] = a1; }
}

__global__ void k_gu(const u16* x, const float* ph, const u16* w1, const u16* w3, float* hid, float* hout) {
  __shared__ float hs[B][D], hn[B][D], red[8], part[4][2][B][64];
  for (int i = threadIdx.x; i < B * D; i += 256) { int b = i / D, c = i % D; float s = 0; for (int hq = 0; hq < HQ; ++hq) s += ph[(hq * B + b) * D + c];
    float hv = rbf(bf2f(x[i]) + rbf(s)); hs[b][c] = hv; if (blockIdx.x == 0) hout[i] = hv; }
  __syncthreads();
  ln_row(hs[0], hn[0], red); ln_row(hs[1], hn[1], red);
  int w = threadIdx.x >> 6, l = threadIdx.x & 63, c = blockIdx.x * 64 + l;
  float g0 = 0, g1 = 0, u0 = 0, u1 = 0;
  for (int k = w * 144; k < w * 144 + 144; ++k) { float a = bf2f(w1[(size_t)k * F + c]), bb = bf2f(w3[(size_t)k * F + c]);
    g0 += hn[0][k] * a; g1 += hn[1][k] * a; u0 += hn[0][k] * bb; u1 += hn[1][k] * bb; }
  part[w][0][0][l] = g0; part[w][0][1][l] = g1; part[w][1][0][l] = u0; part[w][1][1][l] = u1;
  __syncthreads();
  if (threadIdx.x < 128) { int b = threadIdx.x >> 6;
    float g = rbf(part[0][0][b][l] + part[1][0][b][l] + part[2][0][b][l] + part[3][0][b][l]);
    float u = rbf(part[0][1][b][l] + part[1][1][b][l] + part[2][1][b][l] + part[3][1][b][l]);
    float s = rbf(g / (1.f + __expf(-g)));
    hid[b * F + c] = rbf(s * u); }
}

__global__ void k_dn(const float* hid, const float* hin, const u16* w2, __hip_bfloat16* out) {
  __shared__ float part[16][B][64];
  int w = threadIdx.x >> 6, l = threadIdx.x & 63, c = blockIdx.x * 64 + l;
  float a0 = 0, a1 = 0;
  for (int k = w * 288; k < w * 288 + 288; ++k) { float wv = bf2f(w2[(size_t)k * D + c]); a0 += hid[k] * wv; a1 += hid[F + k] * wv; }
  part[w][0][l] = a0; part[w][1][l] = a1;
  __syncthreads();
  if (threadIdx.x < 128) { int b = threadIdx.x >> 6; float s = 0; for (int i = 0; i < 16; ++i) s += part[i][b][l];
    out[b * D + c] = __float2bfloat16(hin[b * D + c] + rbf(s)); }
}

extern "C" void kernel_launch(void* const* d_in, const int* in_sizes, int n_in, void* d_out, int out_size, void* d_ws, size_t ws_size, hipStream_t stream) {
  const u16* x = (const u16*)d_in[0]; const u16* kc = (const u16*)d_in[1]; const u16* vc = (const u16*)d_in[2];
  const u16* wq = (const u16*)d_in[3]; const u16* wo = (const u16*)d_in[4];
  const u16* w1 = (const u16*)d_in[6]; const u16* w3 = (const u16*)d_in[7]; const u16* w2 = (const u16*)d_in[8];
  float* ws = (float*)d_ws;
  float* q = ws; float* po = q + 8192; float* pml = po + 64 * NS * DH; float* ph = pml + 64 * NS * 2; float* hid = ph + 32 * B * D; float* hout = hid + B * F;
  k_q<<<64, 256, 0, stream>>>(x, wq, q);
  k_attn<<<dim3(NS, HKV, B), 256, 0, stream>>>(q, kc, vc, po, pml);
  k_wo<<<32, 576, 0, stream>>>(po, pml, wo, ph);
  k_gu<<<72, 256, 0, stream>>>(x, ph, w1, w3, hid, hout);
  k_dn<<<9, 1024, 0, stream>>>(hid, hout, w2, (__hip_bfloat16*)d_out);
}
```

```cpp
#include <hip/hip_runtime.h>
#include <hip/hip_bf16.h>
#include <stdint.h>

typedef unsigned short u16;
typedef unsigned v4u __attribute__((ext_vector_type(4)));
__device__ __forceinline__ float bf2f(u16 b) { return __uint_as_float(((unsigned)b) << 16); }
__device__ __forceinline__ float bflo(unsigned u) { return __uint_as_float(u << 16); }
__device__ __forceinline__ float bfhi(unsigned u) { return __uint_as_float(u & 0xffff0000u); }
__device__ __forceinline__ float rbf(float f) { return __bfloat162float(__float2bfloat16(f)); }

constexpr int B = 2, D = 576, HQ = 32, HKV = 16, SK = 8192, DH = 128, W = 2048, F = 4608;
constexpr int NS = 8;

__device__ float block_sum(float v, float* red) {
  for (int o = 32; o >= 1; o >>= 1) v += __shfl_xor(v, o);
  int w = threadIdx.x >> 6;
  __syncthreads();
  if ((threadIdx.x & 63) == 0) red[w] = v;
  __syncthreads();
  float s = 0;
  for (int i = 0; i < (int)(blockDim.x >> 6); ++i) s += red[i];
  return s;
}

__device__ void ln_row(const float* src  , float* dst, float* red) {
  float s = 0;
  for (int i = threadIdx.x; i < D; i += blockDim.x) s += src[i];
  float m = block_sum(s, red) * (1.f / D);
  float q = 0;
  for (int i = threadIdx.x; i < D; i += blockDim.x) { float d = src[i] - m; q += d * d; }
  float v = block_sum(q, red) * (1.f / D);
  float r = 1.f / sqrtf(v + 1e-5f);
  for (int i = threadIdx.x; i < D; i += blockDim.x) dst[i] = rbf((src[i] - m) * r);
  __syncthreads();
}

__global__ void k_q(const u16* x, const u16* wq, float* q) {
  __shared__ float xs[B][D], xn[B][D], red[8], part[4][B][64];
  for (int i = threadIdx.x; i < B * D; i += 256) xs[i / D][i % D] = bf2f(x[i]);
  __syncthreads();
  ln_row(xs[0], xn[0], red); ln_row(xs[1], xn[1], red);
  int w = threadIdx.x >> 6, l = threadIdx.x & 63, c = blockIdx.x * 64 + l;
  float a0 = 0, a1 = 0;
  for (int k = w * 144; k < w * 144 + 144; ++k) { float wv = bf2f(wq[(size_t)k * 4096 + c]); a0 += xn[0][k] * wv; a1 += xn[1][k] * wv; }
  part[w][0][l] = a0; part[w][1][l] = a1;
  __syncthreads();
  if (threadIdx.x < 128) { int b = threadIdx.x >> 6; float s = part[0][b][l] + part[1][b][l] + part[2][b][l] + part[3][b][l]; q[b * 4096 + c] = rbf(s); }
}

__global__ void k_attn(const float* q, const u16* kc, const u16* vc, float* po, float* pml) {
  __shared__ float qs[2][DH], p[2][256], red[8];
  int s = blockIdx.x, h = blockIdx.y, b = blockIdx.z, t = threadIdx.x;
  qs[t >> 7][t & 127] = q[b * 4096 + h * 256 + t];
  __syncthreads();
  size_t base = ((size_t)(b * HKV + h) * SK + (SK - W) + s * 256) * DH;
  const v4u* kr = (const v4u*)(kc + base + (size_t)t * DH);
  float s0 = 0, s1 = 0;
  for (int i = 0; i < 16; ++i) { v4u v = kr[i];
    for (int j = 0; j < 4; ++j) { float lo = bflo(v[j]), hi = bfhi(v[j]); int d = i * 8 + j * 2;
      s0 += lo * qs[0][d] + hi * qs[0][d + 1]; s1 += lo * qs[1][d] + hi * qs[1][d + 1]; } }
  s0 *= 0.08838834764831845f; s1 *= 0.08838834764831845f;
  float m0 = s0, m1 = s1;
  for (int o = 32; o >= 1; o >>= 1) { m0 = fmaxf(m0, __shfl_xor(m0, o)); m1 = fmaxf(m1, __shfl_xor(m1, o)); }
  __shared__ float mx[2][4];
  if ((t & 63) == 0) { mx[0][t >> 6] = m0; mx[1][t >> 6] = m1; }
  __syncthreads();
  m0 = fmaxf(fmaxf(mx[0][0], mx[0][1]), fmaxf(mx[0][2], mx[0][3]));
  m1 = fmaxf(fmaxf(mx[1][0], mx[1][1]), fmaxf(mx[1][2], mx[1][3]));
  float p0 = __expf(s0 - m0), p1 = __expf(s1 - m1);
  p[0][t] = p0; p[1][t] = p1;
  float l0 = block_sum(p0, red), l1 = block_sum(p1, red);
  __syncthreads();
  int hh = t >> 7, d = t & 127;
  float acc = 0;
  for (int k = 0; k < 256; ++k) acc += p[hh][k] * bf2f(vc[base + (size_t)k * DH + d]);
  int row = (b * HQ + h * 2 + hh);
  po[((size_t)row * NS + s) * DH + d] = acc;
  if (t == 0) { pml[((b * HQ + h * 2) * NS + s) * 2] = m0; pml[((b * HQ + h * 2) * NS + s) * 2 + 1] = l0;
                pml[((b * HQ + h * 2 + 1) * NS + s) * 2] = m1; pml[((b * HQ + h * 2 + 1) * NS + s) * 2 + 1] = l1; }
}

__global__ void k_wo(const float* po, const float* pml, const u16* wo, float* ph) {
  __shared__ float o[B][DH];
  int hq = blockIdx.x, t = threadIdx.x;
  if (t < 256) { int b = t >> 7, d = t & 127, row = b * HQ + hq;
    float M = -INFINITY; for (int s = 0; s < NS; ++s) M = fmaxf(M, pml[(row * NS + s) * 2]);
    float L = 0, a = 0; for (int s = 0; s < NS; ++s) { float w = __expf(pml[(row * NS + s) * 2] - M); L += w * pml[(row * NS + s) * 2 + 1]; a += w * po[((size_t)row * NS + s) * DH + d]; }
    o[b][d] = rbf(a / L); }
  __syncthreads();
  for (int c = t; c < D; c += blockDim.x) { float a0 = 0, a1 = 0;
    for (int d = 0; d < DH; ++d) { float wv = bf2f(wo[(size_t)(hq * DH + d) * D + c]); a0 += o[0][d] * wv; a1 += o[1][d] * wv; }
    ph[(hq * B + 0) * D + c] = a0; ph[(hq * B + 1) * D + c] = a1; }
}

__global__ void k_gu(const u16* x, const float* ph, const u16* w1, const u16* w3, float* hid, float* hout) {
  __shared__ float hs[B][D], hn[B][D], red[8], part[4][2][B][64];
  for (int i = threadIdx.x; i < B * D; i += 256) { int b = i / D, c = i % D; float s = 0; for (int hq = 0; hq < HQ; ++hq) s += ph[(hq * B + b) * D + c];
    float hv = rbf(bf2f(x[i]) + rbf(s)); hs[b][c] = hv; if (blockIdx.x == 0) hout[i] = hv; }
  __syncthreads();
  ln_row(hs[0], hn[0], red); ln_row(hs[1], hn[1], red);
  int w = threadIdx.x >> 6, l = threadIdx.x & 63, c = blockIdx.x * 64 + l;
  float g0 = 0, g1 = 0, u0 = 0, u1 = 0;
  for (int k = w * 144; k < w * 144 + 144; ++k) { float a = bf2f(w1[(size_t)k * F + c]), bb = bf2f(w3[(size_t)k * F + c]);
    g0 += hn[0][k] * a; g1 += hn[1][k] * a; u0 += hn[0][k] * bb; u1 += hn[1][k] * bb; }
  part[w][0][0][l] = g0; part[w][0][1][l] = g1; part[w][1][0][l] = u0; part[w][1][1][l] = u1;
  __syncthreads();
  if (threadIdx.x < 128) { int b = threadIdx.x >> 6;
    float g = rbf(part[0][0][b][l] + part[1][0][b][l] + part[2][0][b][l] + part[3][0][b][l]);
    float u = rbf(part[0][1][b][l] + part[1][1][b][l] + part[2][1][b][l] + part[3][1][b][l]);
    float s = rbf(g / (1.f + __expf(-g)));
    hid[b * F + c] = rbf(s * u); }
}

__global__ void k_dn(const float* hid, const float* hin, const u16* w2, __hip_bfloat16* out) {
  __shared__ float part[16][B][64];
  int w = threadIdx.x >> 6, l = threadIdx.x & 63, c = blockIdx.x * 64 + l;
  float a0 = 0, a1 = 0;
  for (int k = w * 288; k < w * 288 + 288; ++k) { float wv = bf2f(w2[(size_t)k * D + c]); a0 += hid[k] * wv; a1 += hid[F + k] * wv; }
  part[w][0][l] = a0; part[w][1][l] = a1;
  __syncthreads();
  if (threadIdx.x < 128) { int b = threadIdx.x >> 6; float s = 0; for (int i = 0; i < 16; ++i) s += part[i][b][l];
    out[b * D + c] = __float2bfloat16(hin[b * D + c] + rbf(s)); }
}


#define GAS __attribute__((address_space(1)))
template <int NT>
__global__ void __launch_bounds__(512) k_stream(const v4u* __restrict__ kc, const v4u* __restrict__ vc, unsigned* sink) {
  extern __shared__ char lds_pin[];
  const int w = __builtin_amdgcn_readfirstlane(threadIdx.x >> 6), lane = threadIdx.x & 63;
  const int b = blockIdx.x, r = b >> 3, s = b & 7;
  const size_t base = ((size_t)r * 2097152 + 524288 + (size_t)s * 65536 + ((w + b) & 7) * 8192) / 16;
  const GAS v4u* p = (const GAS v4u*)kc + base + lane;
  const GAS v4u* q = (const GAS v4u*)vc + base + lane;
  v4u v[16];
#pragma unroll
  for (int i = 0; i < 8; ++i) { const GAS v4u* a = p + ((i + r) & 7) * 64; v[i] = NT ? __builtin_nontemporal_load(a) : *a; __builtin_amdgcn_sched_barrier(0); }
#pragma unroll
  for (int i = 0; i < 8; ++i) { const GAS v4u* a = q + ((i + r) & 7) * 64; v[8 + i] = NT ? __builtin_nontemporal_load(a) : *a; __builtin_amdgcn_sched_barrier(0); }
  v4u x = v[0];
#pragma unroll
  for (int i = 1; i < 16; ++i) x ^= v[i];
  unsigned y = x.x ^ x.y ^ x.z ^ x.w;
  if (y == 0x12345678u) sink[threadIdx.x] = y;
}
__global__ void k_empty(unsigned* sink) { if (sink == nullptr) *sink = 1; }
extern "C" void kernel_launch(void* const* d_in, const int* in_sizes, int n_in, void* d_out, int out_size, void* d_ws, size_t ws_size, hipStream_t stream) {
  const u16* x = (const u16*)d_in[0]; const u16* kc = (const u16*)d_in[1]; const u16* vc = (const u16*)d_in[2];
  const u16* wq = (const u16*)d_in[3]; const u16* wo = (const u16*)d_in[4];
  const u16* w1 = (const u16*)d_in[6]; const u16* w3 = (const u16*)d_in[7]; const u16* w2 = (const u16*)d_in[8];
  float* ws = (float*)d_ws;
  float* q = ws; float* po = q + 8192; float* pml = po + 64 * NS * DH; float* ph = pml + 64 * NS * 2; float* hid = ph + 32 * B * D; float* hout = hid + B * F;
  k_empty<<<256, 512, 0, stream>>>((unsigned*)(ws + (1<<20)));
  k_q<<<64, 256, 0, stream>>>(x, wq, q);
  k_attn<<<dim3(NS, HKV, B), 256, 0, stream>>>(q, kc, vc, po, pml);
  k_wo<<<32, 576, 0, stream>>>(po, pml, wo, ph);
  k_gu<<<72, 256, 0, stream>>>(x, ph, w1, w3, hid, hout);
  k_dn<<<9, 1024, 0, stream>>>(hid, hout, w2, (__hip_bfloat16*)d_out);
}
```

```cpp
#include <hip/hip_runtime.h>
#include <stdint.h>

#define GAS __attribute__((address_space(1)))
#define LAS __attribute__((address_space(3)))
typedef unsigned u32;
typedef unsigned long long u64;
typedef unsigned v4u __attribute__((ext_vector_type(4)));
typedef unsigned v2u __attribute__((ext_vector_type(2)));
typedef float v4f __attribute__((ext_vector_type(4)));
typedef float v2f __attribute__((ext_vector_type(2)));
typedef __bf16 v8bf __attribute__((ext_vector_type(8)));
typedef __bf16 v2bf __attribute__((ext_vector_type(2)));
typedef short v4s __attribute__((ext_vector_type(4)));

#define WIN 28
#define WINW 28
#define QTRIG 64
#define STRIG 140
#define PFMODE 0
#define CODE_KB 14

constexpr int WS_EPOCH = 0, WS_ST = 4096, WS_QG = 8192, WS_OG = WS_QG + 131072, WS_RS = WS_OG + 262144, WS_BC = WS_RS + 32 * 4608, BC_STRIDE = 4608,
              WS_H4 = WS_BC + 16 * BC_STRIDE, WS_END = WS_H4 + 144 * 4608;
constexpr int L_T = 0, L_WQ = 65536, L_GP = 73728, L_SP = L_GP + 5120, L_X = 83968, L_JUNK = 87040, L_V = 88064, L_FLAG = L_V + 65536, L_EP = L_FLAG + 64,
              L_QP = L_EP + 256, L_QS = L_QP + 4608, L_P = L_QS + 512, L_WS = L_P + 1024, L_OB = L_WS + 64, L_HW = L_OB + 512, L_FR = L_HW + 128, L_END = L_FR + 768;
static_assert(L_END <= 163840 && L_SP + 2560 <= L_X && L_WQ + 18432 <= L_X, "LDS");
constexpr int F_PROG = 0, F_QP = 1, F_Q = 2, F_QK = 3, F_P = 4, F_PV = 5;
constexpr int NX = 4, N0 = 24, N_K = N0 + 64, N_V = N_K + 64, N_W = N_V + 48, N_T3 = 400;
constexpr int SC1 = 16, NT = 2;
constexpr u32 GSPIN = 1u << 17, LSPIN = 1u << 22;
constexpr int OOB = 0x7ffffff0;
constexpr u32 TMASK = 0x3FFu;

#define STAMP(i)
#define FIN()

#define CBAR() asm volatile("" ::: "memory")
#define PINV(x) asm volatile("" : "+v"(x))
#define PINS(x) asm volatile("" : "+s"(x))
#define GPOLL _Pragma("nounroll") for (u32 sp_ = 0; sp_ < GSPIN; ++sp_)
#define RLX __ATOMIC_RELAXED, __HIP_MEMORY_SCOPE_AGENT
__device__ __forceinline__ void bar() { asm volatile("s_waitcnt lgkmcnt(0)\n\ts_barrier" ::: "memory"); __builtin_amdgcn_sched_barrier(0); }
__device__ __forceinline__ u32 pk(float lo, float hi) { return __builtin_bit_cast(u32, __builtin_convertvector(v2f{lo, hi}, v2bf)); }
__device__ __forceinline__ float asf(u32 u) { return __builtin_bit_cast(float, u); }
__device__ __forceinline__ u32 asu(float f) { return __builtin_bit_cast(u32, f); }
__device__ __forceinline__ v4f asf4(v4u u) { return __builtin_bit_cast(v4f, u); }
__device__ __forceinline__ float lo16(u32 u) { return asf(u << 16); }
__device__ __forceinline__ float hi16(u32 u) { return asf(u & 0xffff0000u); }
__device__ __forceinline__ float rbf(float f) { return lo16(pk(f, f)); }
__device__ __forceinline__ v4f mfma(v4u a, v4u b, v4f c) { return __builtin_amdgcn_mfma_f32_16x16x32_bf16(__builtin_bit_cast(v8bf, a), __builtin_bit_cast(v8bf, b), c, 0, 0, 0); }
__device__ __forceinline__ v4f mfma16(v2u a, v2u b, v4f c) { return __builtin_amdgcn_mfma_f32_16x16x16bf16_1k(__builtin_bit_cast(v4s, a), __builtin_bit_cast(v4s, b), c, 0, 0, 0); }
__device__ __forceinline__ float dot2(u32 a, u32 b, float c) { return __builtin_amdgcn_fdot2_f32_bf16(__builtin_bit_cast(v2bf, a), __builtin_bit_cast(v2bf, b), c, false); }
template <int N> __device__ __forceinline__ float ror(float x) { return asf(__builtin_amdgcn_mov_dpp(asu(x), 0x120 + N, 0xf, 0xf, true)); }
__device__ __forceinline__ float t16(float x, float y) { auto s = __builtin_amdgcn_permlane16_swap(asu(x), asu(y), false, false); return asf(s[0]) + asf(s[1]); }
__device__ __forceinline__ float t32(float x, float y) { auto s = __builtin_amdgcn_permlane32_swap(asu(x), asu(y), false, false); return asf(s[0]) + asf(s[1]); }
__device__ __forceinline__ float tred4(v4f v) { return t16(t32(v[0], v[2]), t32(v[1], v[3])); }
__device__ __forceinline__ float max16x(float x) { auto s = __builtin_amdgcn_permlane16_swap(asu(x), asu(x), false, false); return fmaxf(asf(s[0]), asf(s[1])); }
__device__ __forceinline__ float max32x(float x) { auto s = __builtin_amdgcn_permlane32_swap(asu(x), asu(x), false, false); return fmaxf(asf(s[0]), asf(s[1])); }
__device__ __forceinline__ float rowsum(float x) { x += ror<8>(x); x += ror<4>(x); x += ror<2>(x); x += ror<1>(x); return x; }
__device__ __forceinline__ float rowmax(float x) { x = fmaxf(x, ror<8>(x)); x = fmaxf(x, ror<4>(x)); x = fmaxf(x, ror<2>(x)); x = fmaxf(x, ror<1>(x)); return x; }
__device__ __forceinline__ float ex2(float x) { return __builtin_amdgcn_exp2f(x); }
__device__ __forceinline__ v2u trd(LAS char* p) { return __builtin_bit_cast(v2u, __builtin_amdgcn_ds_read_tr16_b64_v4i16((LAS v4s*)p)); }
__device__ __forceinline__ v4u cat(v2u a, v2u b) { return v4u{a[0], a[1], b[0], b[1]}; }
__device__ __forceinline__ v4u trd2(LAS char* p, int d) { return cat(trd(p), trd(p + d)); }
__device__ __forceinline__ v4f sel4(bool c, v4f a, v4f b) { return v4f{c ? a[0] : b[0], c ? a[1] : b[1], c ? a[2] : b[2], c ? a[3] : b[3]}; }
__device__ __forceinline__ u32 bfi(u32 mask, u32 a, u32 b) { u32 r; asm("v_bfi_b32 %0, %1, %2, %3" : "=v"(r) : "v"(mask), "v"(a), "v"(b)); return r; }
#define ENC(v) (v4u{bfi(tmask, tg[0], asu((v)[0])), bfi(tmask, tg[1], asu((v)[1])), bfi(tmask, tg[2], asu((v)[2])), bfi(tmask, tg[3], asu((v)[3]))})
__device__ __forceinline__ u32 bad(v4u y) { return (y[0] | y[1]) | (y[2] | y[3]); }
#define ALLOK(x) (__builtin_amdgcn_ballot_w64(((x) & TMASK) != 0u) == 0ull)
constexpr float LOG2E = 1.4426950408889634f;
#define ZERO4 (v4f{0.f, 0.f, 0.f, 0.f})

__device__ __forceinline__ void ln_finish(float s, float ss, float& mean_bf, float& sd_bf) {
  float z = rowsum(t16(s, ss));
  auto e = __builtin_amdgcn_permlane16_swap(asu(z), asu(z), false, false);
  const float m = asf(e[0]) * (1.f / 576.f);
  const float var = fmaxf(asf(e[1]) * (1.f / 576.f) - m * m, 0.f);
  mean_bf = rbf(m);
  sd_bf = rbf(__builtin_amdgcn_sqrtf(rbf(rbf(var) + 1e-5f)));
}
#define WPUB(A, val) asm volatile("s_waitcnt vmcnt(%0)\n\tds_write_b32 %1, %2" :: "i"(A), "v"(fl), "v"(val) : "memory")
template <int F, int SLEEP> __device__ __forceinline__ void lwait(u32 fl, u32 n) {
#pragma nounroll
  for (u32 sp = 0; sp < LSPIN; ++sp) {
    u32 v; asm volatile("ds_read_b32 %0, %1 offset:%2\n\ts_waitcnt lgkmcnt(0)" : "=v"(v) : "v"(fl), "i"(4 * F) : "memory");
    if ((u32)__builtin_amdgcn_readfirstlane(v) >= n) break;
    if (SLEEP) __builtin_amdgcn_s_sleep(SLEEP);
  }
  __builtin_amdgcn_sched_barrier(0);
}
#define LWAIT(F, n) lwait<F, 1>(fl, (u32)(n))
#define LWAIT0(F, n) lwait<F, 0>(fl, (u32)(n))
#define LINC(F) asm volatile("s_mov_b64 exec, 1\n\tds_add_u32 %0, %1 offset:%2\n\ts_mov_b64 exec, -1" :: "v"(fl), "v"(one), "i"(4 * (F)) : "memory")
#define DMA0(src, dst, imm) __builtin_amdgcn_global_load_lds((const GAS u32*)(src), (LAS u32*)(L + (dst)), 16, imm, 0)
#define DMA(src, dst, imm) __builtin_amdgcn_global_load_lds((const GAS u32*)(src), (LAS u32*)(L + (dst)), 16, imm, NT)
#define LDU(off, soff) __builtin_amdgcn_raw_buffer_load_b128(rws, (off), (soff), SC1)
#define STU(v, off, soff) __builtin_amdgcn_raw_buffer_store_b128((v), rws, (off), (soff), SC1)
__device__ __forceinline__ v4u make_tag(u32 ep) { const u32 t = (ep + 1u) * 0x2F5u; return v4u{t & TMASK, (t + 341u) & TMASK, (t + 682u) & TMASK, (t + 1023u) & TMASK}; }

__global__ void __launch_bounds__(640) fused_decode(const char* __restrict__ x, char* __restrict__ ws, const char* __restrict__ wq, const char* __restrict__ kc,
                                                    const char* __restrict__ vc, const char* __restrict__ wo, const char* __restrict__ w1,
                                                    const char* __restrict__ w3, const char* __restrict__ w2, char* __restrict__ out) {
  extern __shared__ __attribute__((aligned(16))) char lds_[];
  LAS char* L = (LAS char*)lds_;
  u64 pc0; asm volatile("s_getpc_b64 %0" : "=s"(pc0));
  const int l = threadIdx.x & 63, wid = __builtin_amdgcn_readfirstlane(threadIdx.x >> 6), w = wid - 1;
  const int c = blockIdx.x, xcd = c & 7, t = c >> 3, g = xcd * 2 + (t >> 4), u = t & 15, b = u >> 3, j = u & 7;
  const int grp = l >> 4;
  const bool isM = u < 9, isW = u >= 9 && u < 15;
  const int m = g * 9 + u, r = g * 7 + (u - 9), hh = u >= 12 ? 1 : 0, cb3 = u - 9 - 3 * hh;
  const bool isR = !isM && r < 72;
  const int rot = (c + (c >> 4)) & 15, rot8 = rot >> 1;
  u32 fl = L_FLAG;
  STAMP(0);

  if (__builtin_expect(wid == 0, 1)) {
    __builtin_amdgcn_s_setprio(3);
    const u32 p8r = l >> 3, p8c = ((l & 7) ^ ((l >> 3) & 2)) << 4;
    {
      DMA0((const GAS char*)x + l * 16, L_X, 0); DMA0((const GAS char*)x + l * 16, L_X, 1024); DMA0((const GAS char*)x + (l & 15) * 16, L_X, 2048);
      __builtin_amdgcn_global_load_lds((const GAS u32*)(ws + WS_EPOCH), (LAS u32*)(L + L_EP), 4, 0, SC1);
      DMA0((pc0 & ~255ull) + (u < CODE_KB ? u : CODE_KB - 1) * 1024 + l * 16, L_JUNK, 0);
      const GAS char* qp = (const GAS char*)wq + (size_t)((xcd >> 1) * 144) * 8192 + t * 256 + (xcd & 1) * 128;
      const u32 o = p8r * 8192 + p8c, o1 = (o ^ 64) + 65536 - 1024;
#pragma unroll 1
      for (int i = 0; i < 9; ++i) { const GAS char* s_ = qp + i * 131072; DMA(s_ + o, L_WQ + i * 2048, 0); DMA(s_ + o1, L_WQ + i * 2048, 1024); }
      __builtin_amdgcn_global_load_lds((const GAS u32*)x, (LAS u32*)(L + L_JUNK), 4, 0, 0);
    }
    const size_t reg = ((size_t)(b * 16 + g) * 8192 + 6144 + j * 256) * 256;
    const GAS char* kp = (const GAS char*)kc + reg;
    const u32 kv0 = grp * 256 + (((l & 15) ^ grp) << 4);
#define KGRP(gi) do { const u32 go_ = (((gi) + rot) & 15) * 4096; const GAS char* s_ = kp + go_; \
      DMA(s_ + kv0, L_T + go_, 0); DMA(s_ + (kv0 ^ 64), L_T + go_, 1024); DMA(s_ + (kv0 ^ 128), L_T + go_, 2048); DMA(s_ + (kv0 ^ 192), L_T + go_, 3072); } while (0)
    KGRP(0);
    if (l < 16) asm volatile("ds_write_b32 %0, %1" :: "v"(L_FLAG + l * 4), "v"(0) : "memory");
    bar();
    WPUB(N0 + 4 - NX, NX);
    STAMP(3);
#define PACE(n) do { if (WINW != WIN && isW) WPUB(WINW - 4, (n) - WINW + 4 > NX ? (n) - WINW + 4 : NX); else WPUB(WIN - 4, (n) - WIN + 4 > NX ? (n) - WIN + 4 : NX); } while (0)
#pragma unroll 1
    for (int gi = 1; gi < 16; ++gi) { PACE(N0 + gi * 4); KGRP(gi); }
    STAMP(4);
    const GAS char* vp = (const GAS char*)vc + reg;
    const u32 vv0 = grp * 256 + (((l & 15) ^ (grp << 2)) << 4);
#pragma unroll 1
    for (int gi = 0; gi < 16; ++gi) {
      PACE(N_K + gi * 4);
      const GAS char* s = vp + (((gi + 2 * rot8) & 15) ^ (rot & 1)) * 4096; const int d = L_V + (gi ^ (rot & 1)) * 4096;
      DMA(s + vv0, d, 0); DMA(s + (vv0 ^ 16), d, 1024); DMA(s + (vv0 ^ 32), d, 2048); DMA(s + (vv0 ^ 48), d, 3072);
    }
    STAMP(5);
    const __amdgpu_buffer_rsrc_t rws = __builtin_amdgcn_make_buffer_rsrc(ws, 0, WS_END, 0x00020000);
    const u32 o2 = p8r * 1152 + p8c;
    LWAIT0(F_QP, 9);
    if (isM) {
      const u32 o13 = (l >> 2) * 9216 + (((l & 3) ^ ((l >> 4) & 2)) << 4) + m * 64;
#define STRIP(rg, dst) do { DMA((const GAS char*)w1 + (size_t)(rg) * 147456 + o13, (dst), 0); DMA((const GAS char*)w3 + (size_t)(rg) * 147456 + o13, (dst) + ((rg) < 18 ? -1024 : 1024), 0); } while (0)
      LWAIT0(F_QK, 4);
      WPUB(24, N_V - 24); WPUB(16, N_V - 16); STRIP(0, L_T + 71 * 1024); STRIP(1, L_T + 69 * 1024);
      WPUB(12, N_V - 8); STRIP(2, L_T + 67 * 1024); STRIP(3, L_T + 65 * 1024);
      WPUB(8, N_V);
      STAMP(6);
      STRIP(4, L_T + 63 * 1024); STRIP(5, L_T + 61 * 1024);
      LWAIT0(F_PV, 4);
      constexpr int RG0 = 6;
#pragma unroll 1
      for (int rg = RG0; rg < 18; ++rg) { asm volatile("s_waitcnt vmcnt(56)" ::: "memory"); STRIP(rg, L_T + (71 - 2 * rg) * 1024); }
#pragma unroll 1
      for (int rg = 18; rg < 36; ++rg) { asm volatile("s_waitcnt vmcnt(56)" ::: "memory"); STRIP(rg, L_V + (2 * rg - 36) * 1024); }
      const GAS char* dp = (const GAS char*)w2 + (size_t)m * 36864;
#pragma unroll 1
      for (int i = 0; i < 9; ++i) {
        asm volatile("s_waitcnt vmcnt(56)" ::: "memory");
        const GAS char* s = dp + i * 128;
        DMA(s + o2, L_T + i * 4096, 0); DMA(s + ((o2 ^ 64) + 9216 - 1024), L_T + i * 4096, 1024);
        DMA(s + (o2 + 2 * 9216 - 2048), L_T + i * 4096, 2048); DMA(s + ((o2 ^ 64) + 3 * 9216 - 3072), L_T + i * 4096, 3072);
      }
      WPUB(36, N_T3);
      STAMP(10);
      asm volatile("s_waitcnt vmcnt(0)" ::: "memory");
      STAMP(11);
      v4u tg = make_tag(*(LAS u32*)(L + L_EP));
      int xso = (l >> 5) * 2304 + (l & 31) * 16, xso4 = (l >> 5) * 2304 + (l & 15) * 16 + 2048, bo = WS_BC + (xcd * 2 + (t & 1)) * BC_STRIDE;
      LAS char* gpp = L + L_GP + l * 80; LAS char* hwp = L + L_HW + l * 2;
      bar();
      v2f cs2;
      {
        const LAS v4f* sp = (const LAS v4f*)(L + L_SP + (l & 31) * 80);
        const v4f s = (sp[0] + sp[1]) + (sp[2] + sp[3]);
        const v2f s8 = *(const LAS v2f*)(sp + 4);
        cs2 = v2f{s[0] + s[2] + s8[0], s[1] + s[3] + s8[1]};
      }
      PINV(cs2); PINV(tg); PINV(xso); PINV(xso4); PINS(bo); PINV(gpp); PINV(hwp);
      v4u hx[5];
      GPOLL {
        CBAR();
#pragma unroll
        for (int i = 0; i < 4; ++i) hx[i] = LDU(xso + i * 512, bo) ^ tg;
        hx[4] = LDU(xso4, bo) ^ tg;
        if (ALLOK((bad(hx[0]) | bad(hx[1])) | (bad(hx[2]) | bad(hx[3])) | bad(hx[4]))) break;
      }
      STAMP(12);
      if (l & 16) hx[4] = v4u{0u, 0u, 0u, 0u};
      v4f s4 = asf4(hx[0]), q4 = s4 * s4;
#pragma unroll
      for (int i = 1; i < 5; ++i) { const v4f v = asf4(hx[i]); s4 += v; q4 += v * v; }
      float mb, sd; ln_finish((s4[0] + s4[1]) + (s4[2] + s4[3]), (q4[0] + q4[1]) + (q4[2] + q4[3]), mb, sd);
      float rsd = __builtin_amdgcn_rcpf(sd);
      v2f off = cs2 * mb;
      PINV(rsd); PINV(off);
      bar();
      STAMP(13);
      {
        const LAS v4f* gp4 = (const LAS v4f*)gpp;
        const v4f s = (gp4[0] + gp4[1]) + (gp4[2] + gp4[3]);
        const v2f s8 = *(const LAS v2f*)(gp4 + 4) - off;
        const u32 gu = pk((s[0] + s[2] + s8[0]) * rsd, (s[1] + s[3] + s8[1]) * rsd);
        const float gg = lo16(gu), uu = hi16(gu);
        const float hd = rbf(gg * __builtin_amdgcn_rcpf(1.f + ex2(-gg * LOG2E))) * uu;
        *(LAS unsigned short*)hwp = (unsigned short)pk(hd, hd);
      }
      bar();
      STAMP(14);
      FIN(); return;
    }
    if (isW) {
      const GAS char* op = (const GAS char*)wo + (size_t)((g * 2 + hh) * 128) * 1152 + cb3 * 384;
      LWAIT0(F_QK, 4);
#pragma unroll 1
      for (int gi = 0; gi < 12; ++gi) {
        WPUB(WINW - 4, N_V + gi * 4 - WINW + 4);
        const GAS char* s = op + ((gi & 3) * 36864 + (gi >> 2) * 128);
        const int d = L_T + (68 - 4 * gi) * 1024;
        DMA(s + o2, d, 0); DMA(s + ((o2 ^ 64) + 9216 - 1024), d, 1024); DMA(s + (o2 + 2 * 9216 - 2048), d, 2048); DMA(s + ((o2 ^ 64) + 3 * 9216 - 3072), d, 3072);
      }
      WPUB(0, N_W);
    } else {
      WPUB(24, N_V - 24); WPUB(16, N_V - 16); WPUB(8, N_V - 8); WPUB(0, N_V);
    }
    STAMP(11);
    if (!isR) { FIN(); return; }
    const u32 ep = *(LAS u32*)(L + L_EP);
    v4u tg = make_tag(ep);
    float xf = lo16(*(LAS unsigned short*)(L + L_X + r * 32 + (l & 3) * 8 + grp * 2));
    int go = (l >> 2) * 4608 + r * 64 + (l & 3) * 16;
    LAS char* frp = L + L_FR + (4 * (l & 3) + grp) * 48;
    GAS char* outp = (GAS char*)out + r * 32 + (l & 3) * 8 + grp * 2;
    PINV(tg); PINV(xf); PINV(go); PINV(frp); PINV(outp);
    if (isW) bar();
    float hv;
    {
      v4u p0, p1;
      GPOLL {
        CBAR();
        p0 = LDU(go, WS_RS) ^ tg; p1 = LDU(go, WS_RS + 16 * 4608) ^ tg;
        if (ALLOK(bad(p0) | bad(p1))) break;
      }
      STAMP(10);
      float s = tred4(asf4(p0) + asf4(p1));
      s += ror<4>(s); s += ror<8>(s);
      hv = rbf(xf + rbf(s));
      auto e = __builtin_amdgcn_permlane16_swap(asu(hv), asu(hv), false, false);
      auto e0 = __builtin_amdgcn_permlane32_swap(e[0], e[0], false, false);
      auto e1 = __builtin_amdgcn_permlane32_swap(e[1], e[1], false, false);
      STU((v4u{e0[0], e1[0], e0[1], e1[1]}) | tg, go, WS_BC);
      STAMP(9);
    }
    bar();
    bar();
    STAMP(14);
    {
      const v4f a0 = *(LAS v4f*)frp, a1 = *(LAS v4f*)(frp + 16); const float a2 = *(LAS float*)(frp + 32);
      const v4f a = a0 + a1;
      const float ov = hv + rbf(((a[0] + a[1]) + (a[2] + a[3])) + a2);
      if ((l & 12) == 0) *(GAS unsigned short*)outp = (unsigned short)pk(ov, ov);
      if (r == 0 && l == 0) __hip_atomic_store((GAS u32*)(ws + WS_EPOCH), ep + 1u, RLX);
    }
    STAMP(15);
    FIN(); return;
  }

  asm volatile("s_barrier" ::: "memory");
  __builtin_amdgcn_sched_barrier(0);
  const __amdgpu_buffer_rsrc_t rws = __builtin_amdgcn_make_buffer_rsrc(ws, 0, WS_END, 0x00020000);
  const int tq = (l >> 2) & 3, tp = (l & 3) * 8;
  u32 one = 1u, tmask = TMASK;
  PINV(fl); PINV(one); PINV(tmask);
  LWAIT(F_PROG, NX);
  v4u tg = make_tag(*(LAS u32*)(L + L_EP));
  PINV(tg);
  {
    const int rs = xcd >> 1, cbq = 2 * (t & 1) + (xcd & 1), gq_ = t >> 1;
    v4u xs[3];
    {
      LAS char* xp = L + L_X + (l >> 5) * 1152 + (l & 31) * 16;
      xs[0] = *(LAS v4u*)xp; xs[1] = *(LAS v4u*)(xp + 512);
      const v4u t2 = *(LAS v4u*)(xp + 1024 - ((l & 31) < 8 ? 0 : 1024));
      xs[2] = (l & 31) < 8 ? t2 : v4u{0u, 0u, 0u, 0u};
    }
    const v2u xa = *(LAS v2u*)(L + L_X + (l & 1) * 1152 + (rs * 144 + 16 * w + 4 * grp) * 2);
    float s = 0.f, ss = 0.f;
#pragma unroll
    for (int i = 0; i < 3; ++i)
#pragma unroll
      for (int k = 0; k < 4; ++k) { s = dot2(xs[i][k], 0x3F803F80u, s); ss = dot2(xs[i][k], xs[i][k], ss); }
    float mb, sd; ln_finish(s, ss, mb, sd);
    const float m0 = asf(__builtin_amdgcn_readlane(asu(mb), 0)), m1 = asf(__builtin_amdgcn_readlane(asu(mb), 32));
    const float d0 = asf(__builtin_amdgcn_readlane(asu(sd), 0)), d1 = asf(__builtin_amdgcn_readlane(asu(sd), 32));
    const float mm = (l & 1) ? m1 : m0, dd = __builtin_amdgcn_rcpf((l & 1) ? d1 : d0);
    v2u a;
#pragma unroll
    for (int k = 0; k < 2; ++k) { const u32 dlt = pk(lo16(xa[k]) - mm, hi16(xa[k]) - mm); a[k] = pk(lo16(dlt) * dd, hi16(dlt) * dd); }
    LAS char* qb = L + L_WQ + (2 * w + (grp >> 1)) * 1024 + (4 * (grp & 1) + tq) * 128 + tp;
    const int sw = ((tq >> 1) | (grp & 2)) << 5;
    LAS char* qb0 = qb + sw; LAS char* qb1 = qb + (32 ^ sw); LAS char* qb2 = qb + (64 ^ sw); LAS char* qb3 = qb + (96 ^ sw);
    LAS char* qw = L + L_QP + w * 512 + (l & 1) * 256 + (l & 6) * 32 + grp * 16;
    PINV(a); PINV(qb0); PINV(qb1); PINV(qb2); PINV(qb3); PINV(qw);
    LWAIT(F_PROG, 2 * w + 7);
    const v4f q0 = mfma16(trd(qb0), a, ZERO4), q1 = mfma16(trd(qb1), a, ZERO4), q2 = mfma16(trd(qb2), a, ZERO4), q3 = mfma16(trd(qb3), a, ZERO4);
    *(LAS v4f*)qw = sel4((l & 4) != 0, sel4((l & 2) != 0, q3, q2), sel4((l & 2) != 0, q1, q0));
    LINC(F_QP);
    STAMP(1);
    if (w == 8) {
      LAS char* qr = L + L_QP + (l & 31) * 16;
      int so = l < 32 ? grp * 4096 + (l & 15) * 16 : OOB, sb = WS_QG + gq_ * 8192 + rs * 1024 + cbq * 256;
      PINV(qr); PINV(so); PINS(sb);
      LWAIT0(F_QP, 9);
      const LAS v4f* q4p = (const LAS v4f*)qr;
      const v4f q4 = (((q4p[0] + q4p[32]) + (q4p[64] + q4p[96])) + ((q4p[128] + q4p[160]) + (q4p[192] + q4p[224]))) + q4p[256];
      const v4f q4r = asf4(__builtin_bit_cast(v4u, q4) + 0x200u);
      STU(ENC(q4r), so, sb);
      STAMP(2);
      int lo = l * 16, lb = WS_QG + g * 8192 + b * 4096;
      LAS char* qs = L + L_QS + l * 8;
      PINV(lo); PINS(lb); PINV(qs);
      LWAIT(F_PROG, QTRIG);
      v4u gq[4];
      GPOLL {
        CBAR();
#pragma unroll
        for (int i = 0; i < 4; ++i) gq[i] = LDU(lo + i * 1024, lb) ^ tg;
        if (ALLOK((bad(gq[0]) | bad(gq[1])) | (bad(gq[2]) | bad(gq[3])))) break;
      }
      const v4f qv = (asf4(gq[0]) + asf4(gq[1])) + (asf4(gq[2]) + asf4(gq[3]));
      *(LAS v2u*)qs = v2u{pk(qv[0], qv[1]), pk(qv[2], qv[3])};
      LINC(F_Q);
      STAMP(3);
      LWAIT(F_P, 4);
      const v4f ma = *(LAS v4f*)(L + L_WS + (l >> 5) * 32), la = *(LAS v4f*)(L + L_WS + (l >> 5) * 32 + 16);
      const float mc = fmaxf(fmaxf(ma[0], ma[1]), fmaxf(ma[2], ma[3]));
      const float lt = (la[0] * ex2((ma[0] - mc) * LOG2E) + la[1] * ex2((ma[1] - mc) * LOG2E)) + (la[2] * ex2((ma[2] - mc) * LOG2E) + la[3] * ex2((ma[3] - mc) * LOG2E));
      const v4f sq = {mc, lt, asf(__builtin_amdgcn_readlane(asu(mc), 32)), asf(__builtin_amdgcn_readlane(asu(lt), 32))};
      STU(ENC(sq), l == 0 ? 0 : OOB, WS_ST + g * 256 + u * 16);
      STAMP(5);
    }
  }
  if (w >= 4 && w < 8) {
    const int v = w - 4;
    const int kx = (l & 15) * 256 + ((grp ^ (l & 15)) << 4);
    const int ka = L_T + ((2 * v + rot8) & 7) * 8192 + kx, kb = L_T + ((2 * v + 1 + rot8) & 7) * 8192 + kx;
    LAS char* qa = L + L_QS + (l & 1) * 256 + grp * 16;
    LAS char* pw = L + L_P + (l >> 5) * 512 + v * 128 + (l & 31) * 2;
    LAS char* sp = L + L_WS + (l >> 5) * 32 + v * 4;
    PINV(qa); PINV(pw); PINV(sp);
    LWAIT(F_PROG, N_K);
    v4u kf[4][4];
#pragma unroll
    for (int ds = 0; ds < 4; ++ds) {
      kf[ds][0] = *(LAS v4u*)(L + (ka ^ (ds << 6))); kf[ds][1] = *(LAS v4u*)(L + (ka ^ (ds << 6)) + 4096);
      kf[ds][2] = *(LAS v4u*)(L + (kb ^ (ds << 6))); kf[ds][3] = *(LAS v4u*)(L + (kb ^ (ds << 6)) + 4096);
    }
#pragma unroll
    for (int ds = 0; ds < 4; ++ds) { PINV(kf[ds][0]); PINV(kf[ds][1]); PINV(kf[ds][2]); PINV(kf[ds][3]); }
    LINC(F_QK);
    LWAIT0(F_Q, 1);
    v4u qf[4];
#pragma unroll
    for (int ds = 0; ds < 4; ++ds) qf[ds] = *(LAS v4u*)(qa + ds * 64);
    v4f s[4] = {ZERO4, ZERO4, ZERO4, ZERO4};
#pragma unroll
    for (int ds = 0; ds < 4; ++ds)
#pragma unroll
      for (int kt = 0; kt < 4; ++kt) s[kt] = mfma(qf[ds], kf[ds][kt], s[kt]);
    const bool odd = (grp & 1) != 0, up = l >= 32;
    const float a0 = odd ? s[1][0] : s[0][0], a1 = odd ? s[1][1] : s[0][1], b0 = odd ? s[3][0] : s[2][0], b1 = odd ? s[3][1] : s[2][1];
    const u32 r1 = pk(up ? a1 : a0, up ? b1 : b0);
    const u32 r2 = pk(lo16(r1) * 0.08838834764831845f, hi16(r1) * 0.08838834764831845f);
    const float fa = lo16(r2), fb = hi16(r2);
    const float mx = max16x(rowmax(fmaxf(fa, fb)));
    const float pa = ex2((fa - mx) * LOG2E), pb = ex2((fb - mx) * LOG2E);
    const u32 pp = pk(pa, pb);
    *(LAS unsigned short*)pw = (unsigned short)pp;
    *(LAS unsigned short*)(pw + 64) = (unsigned short)(pp >> 16);
    float ls = rowsum(pa + pb); ls = t16(ls, ls);
    *(LAS float*)sp = mx; *(LAS float*)(sp + 16) = ls;
    LINC(F_P);
    STAMP(5);
  }
  if (w < 4) {
    const int vrow = (8 * grp + tq) * 256 + (l & 1) * 8;
    const int c0 = (4 * w + ((l >> 1) & 1)) ^ (tq << 2) ^ ((2 * grp) & 3);
    LAS char* va00 = L + L_V + vrow + (c0 << 4); LAS char* va01 = L + L_V + vrow + 1024 + ((c0 ^ 1) << 4);
    LAS char* va10 = L + L_V + vrow + ((c0 ^ 2) << 4); LAS char* va11 = L + L_V + vrow + 1024 + ((c0 ^ 3) << 4);
    LAS char* pa = L + L_P + (l & 1) * 512 + grp * 16;
    LAS char* mp = L + L_WS + (l & 1) * 32;
    int so = (l & 12) == 0 ? (l & 1) * 8192 + (l & 2) * 32 + grp * 16 : OOB, sb = WS_OG + g * 16384 + u * 512 + w * 128;
    PINV(va00); PINV(va01); PINV(va10); PINV(va11); PINV(pa); PINV(mp); PINV(so); PINS(sb);
    v4u va[8], vb[8];
#define VREAD(i) do { va[i] = cat(trd(va00 + (i) * 8192), trd(va01 + (i) * 8192)); vb[i] = cat(trd(va10 + (i) * 8192), trd(va11 + (i) * 8192)); } while (0)
    LWAIT(F_PROG, N_K + 32);
    VREAD(0); VREAD(1); VREAD(2); VREAD(3);
    LWAIT0(F_P, 4);
    STAMP(5);
    v4u pf[8];
#pragma unroll
    for (int i = 0; i < 8; ++i) pf[i] = *(LAS v4u*)(pa + i * 64);
    const v4f mg = *(LAS v4f*)mp;
    const float mc = fmaxf(fmaxf(mg[0], mg[1]), fmaxf(mg[2], mg[3]));
    float f0 = ex2((mg[0] - mc) * LOG2E), f1 = ex2((mg[1] - mc) * LOG2E), f2 = ex2((mg[2] - mc) * LOG2E), f3 = ex2((mg[3] - mc) * LOG2E);
    const bool n1 = (l & 2) != 0;
    v4f o = sel4(n1, mfma(vb[1], pf[1], mfma(vb[0], pf[0], ZERO4)), mfma(va[1], pf[1], mfma(va[0], pf[0], ZERO4))) * f0;
    o += sel4(n1, mfma(vb[3], pf[3], mfma(vb[2], pf[2], ZERO4)), mfma(va[3], pf[3], mfma(va[2], pf[2], ZERO4))) * f1;
    PINV(o); PINV(f2); PINV(f3);
    LWAIT0(F_PROG, N_K + 48);
    VREAD(4); VREAD(5);
    o += sel4(n1, mfma(vb[5], pf[5], mfma(vb[4], pf[4], ZERO4)), mfma(va[5], pf[5], mfma(va[4], pf[4], ZERO4))) * f2;
    PINV(o);
    LWAIT0(F_PROG, N_K + 56);
    VREAD(6);
    v4f ta = mfma(va[6], pf[6], ZERO4), tb = mfma(vb[6], pf[6], ZERO4);
    LWAIT0(F_PROG, N_V);
    VREAD(7);
    o += sel4(n1, mfma(vb[7], pf[7], tb), mfma(va[7], pf[7], ta)) * f3;
    STU(ENC(o), so, sb);
    LINC(F_PV);
    STAMP(7);
  }
  const int t8 = tq * 128 + tp, sw8 = ((tq >> 1) | ((grp & 1) << 1)) << 5;
  if (isW) {
    const int hq = g * 2 + hh;
    if (w >= 4 && w < 8) {
      LWAIT(F_PROG, STRIG);
      v4u s0, s1;
      GPOLL {
        CBAR();
        s0 = LDU((l >> 3) * 16, WS_ST + g * 256) ^ tg; s1 = LDU((l >> 3) * 16 + 128, WS_ST + g * 256) ^ tg;
        if (ALLOK(bad(s0) | bad(s1))) break;
      }
      float wn[2];
#pragma unroll
      for (int i = 0; i < 2; ++i) {
        const v4u st = i ? s1 : s0;
        const float mj = asf(hh ? st[2] : st[0]), lj = asf(hh ? st[3] : st[1]);
        float M = fmaxf(mj, ror<8>(mj)); M = max32x(max16x(M));
        const float e = ex2((mj - M) * LOG2E);
        float Ls = e * lj; Ls += ror<8>(Ls); Ls = t16(Ls, Ls); Ls = t32(Ls, Ls);
        wn[i] = e * __builtin_amdgcn_rcpf(Ls);
      }
      int lo = (l >> 3) * 512 + (l & 7) * 16, lb = WS_OG + hq * 8192 + (w - 4) * 128;
      LAS char* ob = L + L_OB + ((l >> 3) & 1) * 256 + (w - 4) * 64 + (l & 7) * 8 + grp * 2;
      PINV(wn[0]); PINV(wn[1]); PINV(lo); PINS(lb); PINV(ob);
      LWAIT0(F_PV, 4);
      v4u o0, o1;
      GPOLL {
        CBAR();
        o0 = LDU(lo, lb) ^ tg; o1 = LDU(lo, lb + 4096) ^ tg;
        if (ALLOK(bad(o0) | bad(o1))) break;
      }
      STAMP(8);
      const float r0 = tred4(asf4(o0) * wn[0]), r1 = tred4(asf4(o1) * wn[1]);
      const bool hi8 = (l & 8) != 0;
      const float val = (hi8 ? r1 : r0) + ror<8>(hi8 ? r0 : r1);
      *(LAS unsigned short*)ob = (unsigned short)pk(val, val);
      bar();
    } else if (w < 4) {
      LAS char* of = L + L_OB + (l & 1) * 256 + grp * 16;
      int so = (l & 15) < 6 ? (l & 1) * 2304 + (l & 6) * 32 + grp * 16 : OOB, sb = WS_RS + hq * 4608 + cb3 * 768 + w * 192;
      PINV(of); PINV(so); PINS(sb);
      LWAIT(F_PROG, N_W);
      v4u wf[3][4];
#pragma unroll
      for (int n = 0; n < 3; ++n) {
        LAS char* wp = L + L_T + (56 - 16 * ((3 * w + n) >> 2) + grp) * 1024 + t8 + ((((3 * w + n) & 3) << 5) ^ sw8);
#pragma unroll
        for (int ks = 0; ks < 4; ++ks) { wf[n][ks] = trd2(wp + (3 - ks) * 4096, 512); PINV(wf[n][ks]); }
      }
      bar();
      v4f y[3] = {ZERO4, ZERO4, ZERO4};
      v4u oq[4];
#pragma unroll
      for (int ks = 0; ks < 4; ++ks) oq[ks] = *(LAS v4u*)(of + ks * 64);
#pragma unroll
      for (int ks = 0; ks < 4; ++ks)
#pragma unroll
        for (int n = 0; n < 3; ++n) y[n] = mfma(wf[n][ks], oq[ks], y[n]);
      const v4f yv = sel4((l & 4) != 0, y[2], sel4((l & 2) != 0, y[1], y[0]));
      STU(ENC(yv), so, sb);
      STAMP(9);
    } else {
      bar();
    }
  }
  if (!isM) {
    if (isR) {
      int go = (l >> 2) * 4608 + r * 64 + (l & 3) * 16, gb = WS_H4 + w * 16 * 4608;
      LAS char* fr = L + L_FR + (4 * (l & 3) + grp) * 48 + w * 4;
      PINV(go); PINS(gb); PINV(fr);
      bar();
      v4u p;
      GPOLL {
        CBAR();
        p = LDU(go, gb) ^ tg;
        if (ALLOK(bad(p))) break;
      }
      STAMP(12);
      float s = tred4(asf4(p));
      s += ror<4>(s); s += ror<8>(s);
      *(LAS float*)fr = s;
      bar();
    }
    FIN(); return;
  }
  {
    LWAIT(F_PROG, N_T3);
    STAMP(11);
    v2u gf[4][2][2];
#pragma unroll
    for (int kb = 0; kb < 4; ++kb) {
      const int rg = 4 * w + kb;
      const int base = (rg < 18 ? L_T + (71 - 2 * rg) * 1024 : L_V + (2 * rg - 36) * 1024) + (4 * grp + tq) * 64 + tp, ms = rg < 18 ? -1024 : 1024;
#pragma unroll
      for (int mat = 0; mat < 2; ++mat)
#pragma unroll
        for (int nt = 0; nt < 2; ++nt) gf[kb][mat][nt] = trd(L + base + mat * ms + ((nt ^ (grp >> 1)) << 5));
    }
    const bool n1 = l >= 32;
    const int grow = (grp & 1) * 32 + (grp >> 1) * 16 + (l & 15);
    {
      const v2u ones = {0x3F803F80u, 0x3F803F80u};
      v4f cs[2][2];
#pragma unroll
      for (int mat = 0; mat < 2; ++mat)
#pragma unroll
        for (int nt = 0; nt < 2; ++nt) cs[mat][nt] = mfma16(ones, gf[3][mat][nt], mfma16(ones, gf[2][mat][nt], mfma16(ones, gf[1][mat][nt], mfma16(ones, gf[0][mat][nt], ZERO4))));
      if ((grp & 1) == 0) *(LAS v2f*)(L + L_SP + grow * 80 + w * 8) = v2f{n1 ? cs[0][1][0] : cs[0][0][0], n1 ? cs[1][1][0] : cs[1][0][0]};
    }
    int ho = ((l >> 2) & 1) * 2304 + (64 * w + 16 * (l & 3) + 4 * grp) * 4, bo = WS_BC + (xcd * 2 + (t & 1)) * BC_STRIDE;
    LAS char* gw = L + L_GP + grow * 80 + w * 8;
    LAS char* hr = L + L_HW + (l & 1) * 64 + grp * 16;
    int so = (l & 8) == 0 ? (l & 1) * 2304 + (l & 6) * 32 + grp * 16 : OOB, sb = WS_H4 + m * 4608 + w * 256;
    PINV(ho); PINS(bo); PINV(gw); PINV(hr); PINV(so); PINS(sb);
    bar();
    v4u df[4];
#pragma unroll
    for (int nt = 0; nt < 4; ++nt) { df[nt] = trd2(L + L_T + (4 * w + grp) * 1024 + t8 + ((nt << 5) ^ sw8), 512); }
    v4u hu;
    GPOLL {
      CBAR();
      hu = LDU(ho, bo);
      if (ALLOK(bad(hu ^ tg))) break;
    }
    STAMP(12);
    const v2u ha = {__builtin_amdgcn_perm(hu[1], hu[0], 0x07060302u), __builtin_amdgcn_perm(hu[3], hu[2], 0x07060302u)};
    float y[2][2];
#pragma unroll
    for (int mat = 0; mat < 2; ++mat)
#pragma unroll
      for (int nt = 0; nt < 2; ++nt) {
        const v4f a0 = mfma16(ha, gf[0][mat][nt], ZERO4), a1 = mfma16(ha, gf[1][mat][nt], ZERO4), a2 = mfma16(ha, gf[2][mat][nt], ZERO4), a3 = mfma16(ha, gf[3][mat][nt], ZERO4);
        y[mat][nt] = (a0[0] + a1[1]) + (a2[2] + a3[3]);
      }
    *(LAS v2f*)gw = v2f{n1 ? y[0][1] : y[0][0], n1 ? y[1][1] : y[1][0]};
    bar();
    STAMP(13);
    bar();
    const v4u da = *(LAS v4u*)hr;
    v4f d[4];
#pragma unroll
    for (int nt = 0; nt < 4; ++nt) d[nt] = mfma(df[nt], da, ZERO4);
    const v4f dv = sel4((l & 4) != 0, sel4((l & 2) != 0, d[3], d[2]), sel4((l & 2) != 0, d[1], d[0]));
    STU(ENC(dv), so, sb);
    STAMP(14);
  }
  FIN();
}

extern "C" void kernel_launch(void* const* d_in, const int* in_sizes, int n_in, void* d_out, int out_size, void* d_ws, size_t ws_size, hipStream_t stream) {
  (void)hipFuncSetAttribute((const void*)fused_decode, hipFuncAttributeMaxDynamicSharedMemorySize, L_END);
  fused_decode<<<256, 640, L_END, stream>>>((const char*)d_in[0], (char*)d_ws, (const char*)d_in[3], (const char*)d_in[1], (const char*)d_in[2], (const char*)d_in[4],
                                           (const char*)d_in[6], (const char*)d_in[7], (const char*)d_in[8], (char*)d_out);
}
```

```cpp
#define STAMPS
#include <cstdio>
#include <hip/hip_runtime.h>
#include <stdint.h>

#define GAS __attribute__((address_space(1)))
#define LAS __attribute__((address_space(3)))
typedef unsigned u32;
typedef unsigned long long u64;
typedef unsigned v4u __attribute__((ext_vector_type(4)));
typedef unsigned v2u __attribute__((ext_vector_type(2)));
typedef float v4f __attribute__((ext_vector_type(4)));
typedef float v2f __attribute__((ext_vector_type(2)));
typedef __bf16 v8bf __attribute__((ext_vector_type(8)));
typedef __bf16 v2bf __attribute__((ext_vector_type(2)));
typedef short v4s __attribute__((ext_vector_type(4)));

#define WIN 28
#define WINW 28
#define QTRIG 64
#define STRIG 140
#define PFMODE 0
#define CODE_KB 14

constexpr int WS_EPOCH = 0, WS_ST = 4096, WS_QG = 8192, WS_OG = WS_QG + 131072, WS_RS = WS_OG + 262144, WS_BC = WS_RS + 32 * 4608, BC_STRIDE = 4608,
              WS_H4 = WS_BC + 16 * BC_STRIDE, WS_END = WS_H4 + 144 * 4608;
constexpr int L_T = 0, L_WQ = 65536, L_GP = 73728, L_SP = L_GP + 5120, L_X = 83968, L_JUNK = 87040, L_V = 88064, L_FLAG = L_V + 65536, L_EP = L_FLAG + 64,
              L_QP = L_EP + 256, L_QS = L_QP + 4608, L_P = L_QS + 512, L_WS = L_P + 1024, L_OB = L_WS + 64, L_HW = L_OB + 512, L_FR = L_HW + 128, L_END = L_FR + 768;
static_assert(L_END <= 163840 && L_SP + 2560 <= L_X && L_WQ + 18432 <= L_X, "LDS");
constexpr int F_PROG = 0, F_QP = 1, F_Q = 2, F_QK = 3, F_P = 4, F_PV = 5;
constexpr int NX = 4, N0 = 24, N_K = N0 + 64, N_V = N_K + 64, N_W = N_V + 48, N_T3 = 400;
constexpr int SC1 = 16, NT = 2;
constexpr u32 GSPIN = 1u << 17, LSPIN = 1u << 22;
constexpr int OOB = 0x7ffffff0;
constexpr u32 TMASK = 0x3FFu;

constexpr size_t STAMP_OFF = 128u << 20;
static const char* stamp_name(int s) { static const char* n[16] = {"start", "qpart", "qpub", "qrdy", "qk", "p", "Vland", "opub", "ordy", "hpub", "rs", "t3", "hrdy", "barB", "dpub", "end"}; return n[s]; }
#define STAMP(i) ts[i] = __builtin_amdgcn_s_memrealtime()
#define FIN() do { if (l == 0) { u64* sp_ = (u64*)(ws + STAMP_OFF) + ((size_t)c * 16 + (wid == 0 ? 9 : wid - 1)) * 16; _Pragma("unroll") for (int i_ = 0; i_ < 16; ++i_) if (ts[i_]) sp_[i_] = ts[i_]; } } while (0)

#define CBAR() asm volatile("" ::: "memory")
#define PINV(x) asm volatile("" : "+v"(x))
#define PINS(x) asm volatile("" : "+s"(x))
#define GPOLL _Pragma("nounroll") for (u32 sp_ = 0; sp_ < GSPIN; ++sp_)
#define RLX __ATOMIC_RELAXED, __HIP_MEMORY_SCOPE_AGENT
__device__ __forceinline__ void bar() { asm volatile("s_waitcnt lgkmcnt(0)\n\ts_barrier" ::: "memory"); __builtin_amdgcn_sched_barrier(0); }
__device__ __forceinline__ u32 pk(float lo, float hi) { return __builtin_bit_cast(u32, __builtin_convertvector(v2f{lo, hi}, v2bf)); }
__device__ __forceinline__ float asf(u32 u) { return __builtin_bit_cast(float, u); }
__device__ __forceinline__ u32 asu(float f) { return __builtin_bit_cast(u32, f); }
__device__ __forceinline__ v4f asf4(v4u u) { return __builtin_bit_cast(v4f, u); }
__device__ __forceinline__ float lo16(u32 u) { return asf(u << 16); }
__device__ __forceinline__ float hi16(u32 u) { return asf(u & 0xffff0000u); }
__device__ __forceinline__ float rbf(float f) { return lo16(pk(f, f)); }
__device__ __forceinline__ v4f mfma(v4u a, v4u b, v4f c) { return __builtin_amdgcn_mfma_f32_16x16x32_bf16(__builtin_bit_cast(v8bf, a), __builtin_bit_cast(v8bf, b), c, 0, 0, 0); }
__device__ __forceinline__ v4f mfma16(v2u a, v2u b, v4f c) { return __builtin_amdgcn_mfma_f32_16x16x16bf16_1k(__builtin_bit_cast(v4s, a), __builtin_bit_cast(v4s, b), c, 0, 0, 0); }
__device__ __forceinline__ float dot2(u32 a, u32 b, float c) { return __builtin_amdgcn_fdot2_f32_bf16(__builtin_bit_cast(v2bf, a), __builtin_bit_cast(v2bf, b), c, false); }
template <int N> __device__ __forceinline__ float ror(float x) { return asf(__builtin_amdgcn_mov_dpp(asu(x), 0x120 + N, 0xf, 0xf, true)); }
__device__ __forceinline__ float t16(float x, float y) { auto s = __builtin_amdgcn_permlane16_swap(asu(x), asu(y), false, false); return asf(s[0]) + asf(s[1]); }
__device__ __forceinline__ float t32(float x, float y) { auto s = __builtin_amdgcn_permlane32_swap(asu(x), asu(y), false, false); return asf(s[0]) + asf(s[1]); }
__device__ __forceinline__ float tred4(v4f v) { return t16(t32(v[0], v[2]), t32(v[1], v[3])); }
__device__ __forceinline__ float max16x(float x) { auto s = __builtin_amdgcn_permlane16_swap(asu(x), asu(x), false, false); return fmaxf(asf(s[0]), asf(s[1])); }
__device__ __forceinline__ float max32x(float x) { auto s = __builtin_amdgcn_permlane32_swap(asu(x), asu(x), false, false); return fmaxf(asf(s[0]), asf(s[1])); }
__device__ __forceinline__ float rowsum(float x) { x += ror<8>(x); x += ror<4>(x); x += ror<2>(x); x += ror<1>(x); return x; }
__device__ __forceinline__ float rowmax(float x) { x = fmaxf(x, ror<8>(x)); x = fmaxf(x, ror<4>(x)); x = fmaxf(x, ror<2>(x)); x = fmaxf(x, ror<1>(x)); return x; }
__device__ __forceinline__ float ex2(float x) { return __builtin_amdgcn_exp2f(x); }
__device__ __forceinline__ v2u trd(LAS char* p) { return __builtin_bit_cast(v2u, __builtin_amdgcn_ds_read_tr16_b64_v4i16((LAS v4s*)p)); }
__device__ __forceinline__ v4u cat(v2u a, v2u b) { return v4u{a[0], a[1], b[0], b[1]}; }
__device__ __forceinline__ v4u trd2(LAS char* p, int d) { return cat(trd(p), trd(p + d)); }
__device__ __forceinline__ v4f sel4(bool c, v4f a, v4f b) { return v4f{c ? a[0] : b[0], c ? a[1] : b[1], c ? a[2] : b[2], c ? a[3] : b[3]}; }
__device__ __forceinline__ u32 bfi(u32 mask, u32 a, u32 b) { u32 r; asm("v_bfi_b32 %0, %1, %2, %3" : "=v"(r) : "v"(mask), "v"(a), "v"(b)); return r; }
#define ENC(v) (v4u{bfi(tmask, tg[0], asu((v)[0])), bfi(tmask, tg[1], asu((v)[1])), bfi(tmask, tg[2], asu((v)[2])), bfi(tmask, tg[3], asu((v)[3]))})
__device__ __forceinline__ u32 bad(v4u y) { return (y[0] | y[1]) | (y[2] | y[3]); }
#define ALLOK(x) (__builtin_amdgcn_ballot_w64(((x) & TMASK) != 0u) == 0ull)
constexpr float LOG2E = 1.4426950408889634f;
#define ZERO4 (v4f{0.f, 0.f, 0.f, 0.f})

__device__ __forceinline__ void ln_finish(float s, float ss, float& mean_bf, float& sd_bf) {
  float z = rowsum(t16(s, ss));
  auto e = __builtin_amdgcn_permlane16_swap(asu(z), asu(z), false, false);
  const float m = asf(e[0]) * (1.f / 576.f);
  const float var = fmaxf(asf(e[1]) * (1.f / 576.f) - m * m, 0.f);
  mean_bf = rbf(m);
  sd_bf = rbf(__builtin_amdgcn_sqrtf(rbf(rbf(var) + 1e-5f)));
}
#define WPUB(A, val) asm volatile("s_waitcnt vmcnt(%0)\n\tds_write_b32 %1, %2" :: "i"(A), "v"(fl), "v"(val) : "memory")
template <int F, int SLEEP> __device__ __forceinline__ void lwait(u32 fl, u32 n) {
#pragma nounroll
  for (u32 sp = 0; sp < LSPIN; ++sp) {
    u32 v; asm volatile("ds_read_b32 %0, %1 offset:%2\n\ts_waitcnt lgkmcnt(0)" : "=v"(v) : "v"(fl), "i"(4 * F) : "memory");
    if ((u32)__builtin_amdgcn_readfirstlane(v) >= n) break;
    if (SLEEP) __builtin_amdgcn_s_sleep(SLEEP);
  }
  __builtin_amdgcn_sched_barrier(0);
}
#define LWAIT(F, n) lwait<F, 1>(fl, (u32)(n))
#define LWAIT0(F, n) lwait<F, 0>(fl, (u32)(n))
#define LINC(F) asm volatile("s_mov_b64 exec, 1\n\tds_add_u32 %0, %1 offset:%2\n\ts_mov_b64 exec, -1" :: "v"(fl), "v"(one), "i"(4 * (F)) : "memory")
#define DMA0(src, dst, imm) __builtin_amdgcn_global_load_lds((const GAS u32*)(src), (LAS u32*)(L + (dst)), 16, imm, 0)
#define DMA(src, dst, imm) __builtin_amdgcn_global_load_lds((const GAS u32*)(src), (LAS u32*)(L + (dst)), 16, imm, NT)
#define LDU(off, soff) __builtin_amdgcn_raw_buffer_load_b128(rws, (off), (soff), SC1)
#define STU(v, off, soff) __builtin_amdgcn_raw_buffer_store_b128((v), rws, (off), (soff), SC1)
__device__ __forceinline__ v4u make_tag(u32 ep) { const u32 t = (ep + 1u) * 0x2F5u; return v4u{t & TMASK, (t + 341u) & TMASK, (t + 682u) & TMASK, (t + 1023u) & TMASK}; }

__global__ void __launch_bounds__(640) fused_decode(const char* __restrict__ x, char* __restrict__ ws, const char* __restrict__ wq, const char* __restrict__ kc,
                                                    const char* __restrict__ vc, const char* __restrict__ wo, const char* __restrict__ w1,
                                                    const char* __restrict__ w3, const char* __restrict__ w2, char* __restrict__ out) {
  extern __shared__ __attribute__((aligned(16))) char lds_[];
  LAS char* L = (LAS char*)lds_;
  u64 pc0; asm volatile("s_getpc_b64 %0" : "=s"(pc0));
  const int l = threadIdx.x & 63, wid = __builtin_amdgcn_readfirstlane(threadIdx.x >> 6), w = wid - 1;
  const int c = blockIdx.x, xcd = c & 7, t = c >> 3, g = xcd * 2 + (t >> 4), u = t & 15, b = u >> 3, j = u & 7;
  const int grp = l >> 4;
  const bool isM = u < 9, isW = u >= 9 && u < 15;
  const int m = g * 9 + u, r = g * 7 + (u - 9), hh = u >= 12 ? 1 : 0, cb3 = u - 9 - 3 * hh;
  const bool isR = !isM && r < 72;
  const int rot = (c + (c >> 4)) & 15, rot8 = rot >> 1;
  u32 fl = L_FLAG;
  u64 ts[16];
#pragma unroll
  for (int i = 0; i < 16; ++i) ts[i] = 0;
  STAMP(0);

  if (__builtin_expect(wid == 0, 1)) {
    __builtin_amdgcn_s_setprio(3);
    const u32 p8r = l >> 3, p8c = ((l & 7) ^ ((l >> 3) & 2)) << 4;
    {
      DMA0((const GAS char*)x + l * 16, L_X, 0); DMA0((const GAS char*)x + l * 16, L_X, 1024); DMA0((const GAS char*)x + (l & 15) * 16, L_X, 2048);
      __builtin_amdgcn_global_load_lds((const GAS u32*)(ws + WS_EPOCH), (LAS u32*)(L + L_EP), 4, 0, SC1);
      DMA0((pc0 & ~255ull) + (u < CODE_KB ? u : CODE_KB - 1) * 1024 + l * 16, L_JUNK, 0);
      const GAS char* qp = (const GAS char*)wq + (size_t)((xcd >> 1) * 144) * 8192 + t * 256 + (xcd & 1) * 128;
      const u32 o = p8r * 8192 + p8c, o1 = (o ^ 64) + 65536 - 1024;
#pragma unroll 1
      for (int i = 0; i < 9; ++i) { const GAS char* s_ = qp + i * 131072; DMA(s_ + o, L_WQ + i * 2048, 0); DMA(s_ + o1, L_WQ + i * 2048, 1024); }
      __builtin_amdgcn_global_load_lds((const GAS u32*)x, (LAS u32*)(L + L_JUNK), 4, 0, 0);
    }
    const size_t reg = ((size_t)(b * 16 + g) * 8192 + 6144 + j * 256) * 256;
    const GAS char* kp = (const GAS char*)kc + reg;
    const u32 kv0 = grp * 256 + (((l & 15) ^ grp) << 4);
#define KGRP(gi) do { const u32 go_ = (((gi) + rot) & 15) * 4096; const GAS char* s_ = kp + go_; \
      DMA(s_ + kv0, L_T + go_, 0); DMA(s_ + (kv0 ^ 64), L_T + go_, 1024); DMA(s_ + (kv0 ^ 128), L_T + go_, 2048); DMA(s_ + (kv0 ^ 192), L_T + go_, 3072); } while (0)
    KGRP(0);
    if (l < 16) asm volatile("ds_write_b32 %0, %1" :: "v"(L_FLAG + l * 4), "v"(0) : "memory");
    bar();
    WPUB(N0 + 4 - NX, NX);
    STAMP(3);
#define PACE(n) do { if (WINW != WIN && isW) WPUB(WINW - 4, (n) - WINW + 4 > NX ? (n) - WINW + 4 : NX); else WPUB(WIN - 4, (n) - WIN + 4 > NX ? (n) - WIN + 4 : NX); } while (0)
#pragma unroll 1
    for (int gi = 1; gi < 16; ++gi) { PACE(N0 + gi * 4); KGRP(gi); }
    STAMP(4);
    const GAS char* vp = (const GAS char*)vc + reg;
    const u32 vv0 = grp * 256 + (((l & 15) ^ (grp << 2)) << 4);
#pragma unroll 1
    for (int gi = 0; gi < 16; ++gi) {
      PACE(N_K + gi * 4);
      const GAS char* s = vp + (((gi + 2 * rot8) & 15) ^ (rot & 1)) * 4096; const int d = L_V + (gi ^ (rot & 1)) * 4096;
      DMA(s + vv0, d, 0); DMA(s + (vv0 ^ 16), d, 1024); DMA(s + (vv0 ^ 32), d, 2048); DMA(s + (vv0 ^ 48), d, 3072);
    }
    STAMP(5);
    const __amdgpu_buffer_rsrc_t rws = __builtin_amdgcn_make_buffer_rsrc(ws, 0, WS_END, 0x00020000);
    const u32 o2 = p8r * 1152 + p8c;
    LWAIT0(F_QP, 9);
    if (isM) {
      const u32 o13 = (l >> 2) * 9216 + (((l & 3) ^ ((l >> 4) & 2)) << 4) + m * 64;
#define STRIP(rg, dst) do { DMA((const GAS char*)w1 + (size_t)(rg) * 147456 + o13, (dst), 0); DMA((const GAS char*)w3 + (size_t)(rg) * 147456 + o13, (dst) + ((rg) < 18 ? -1024 : 1024), 0); } while (0)
      LWAIT0(F_QK, 4);
      WPUB(24, N_V - 24); WPUB(16, N_V - 16); STRIP(0, L_T + 71 * 1024); STRIP(1, L_T + 69 * 1024);
      WPUB(12, N_V - 8); STRIP(2, L_T + 67 * 1024); STRIP(3, L_T + 65 * 1024);
      WPUB(8, N_V);
      STAMP(6);
      STRIP(4, L_T + 63 * 1024); STRIP(5, L_T + 61 * 1024);
      LWAIT0(F_PV, 4);
      constexpr int RG0 = 6;
#pragma unroll 1
      for (int rg = RG0; rg < 18; ++rg) { asm volatile("s_waitcnt vmcnt(56)" ::: "memory"); STRIP(rg, L_T + (71 - 2 * rg) * 1024); }
#pragma unroll 1
      for (int rg = 18; rg < 36; ++rg) { asm volatile("s_waitcnt vmcnt(56)" ::: "memory"); STRIP(rg, L_V + (2 * rg - 36) * 1024); }
      const GAS char* dp = (const GAS char*)w2 + (size_t)m * 36864;
#pragma unroll 1
      for (int i = 0; i < 9; ++i) {
        asm volatile("s_waitcnt vmcnt(56)" ::: "memory");
        const GAS char* s = dp + i * 128;
        DMA(s + o2, L_T + i * 4096, 0); DMA(s + ((o2 ^ 64) + 9216 - 1024), L_T + i * 4096, 1024);
        DMA(s + (o2 + 2 * 9216 - 2048), L_T + i * 4096, 2048); DMA(s + ((o2 ^ 64) + 3 * 9216 - 3072), L_T + i * 4096, 3072);
      }
      WPUB(36, N_T3);
      STAMP(10);
      asm volatile("s_waitcnt vmcnt(0)" ::: "memory");
      STAMP(11);
      v4u tg = make_tag(*(LAS u32*)(L + L_EP));
      int xso = (l >> 5) * 2304 + (l & 31) * 16, xso4 = (l >> 5) * 2304 + (l & 15) * 16 + 2048, bo = WS_BC + (xcd * 2 + (t & 1)) * BC_STRIDE;
      LAS char* gpp = L + L_GP + l * 80; LAS char* hwp = L + L_HW + l * 2;
      bar();
      v2f cs2;
      {
        const LAS v4f* sp = (const LAS v4f*)(L + L_SP + (l & 31) * 80);
        const v4f s = (sp[0] + sp[1]) + (sp[2] + sp[3]);
        const v2f s8 = *(const LAS v2f*)(sp + 4);
        cs2 = v2f{s[0] + s[2] + s8[0], s[1] + s[3] + s8[1]};
      }
      PINV(cs2); PINV(tg); PINV(xso); PINV(xso4); PINS(bo); PINV(gpp); PINV(hwp);
      v4u hx[5];
      GPOLL {
        CBAR();
#pragma unroll
        for (int i = 0; i < 4; ++i) hx[i] = LDU(xso + i * 512, bo) ^ tg;
        hx[4] = LDU(xso4, bo) ^ tg;
        if (ALLOK((bad(hx[0]) | bad(hx[1])) | (bad(hx[2]) | bad(hx[3])) | bad(hx[4]))) break;
      }
      STAMP(12);
      if (l & 16) hx[4] = v4u{0u, 0u, 0u, 0u};
      v4f s4 = asf4(hx[0]), q4 = s4 * s4;
#pragma unroll
      for (int i = 1; i < 5; ++i) { const v4f v = asf4(hx[i]); s4 += v; q4 += v * v; }
      float mb, sd; ln_finish((s4[0] + s4[1]) + (s4[2] + s4[3]), (q4[0] + q4[1]) + (q4[2] + q4[3]), mb, sd);
      float rsd = __builtin_amdgcn_rcpf(sd);
      v2f off = cs2 * mb;
      PINV(rsd); PINV(off);
      bar();
      STAMP(13);
      {
        const LAS v4f* gp4 = (const LAS v4f*)gpp;
        const v4f s = (gp4[0] + gp4[1]) + (gp4[2] + gp4[3]);
        const v2f s8 = *(const LAS v2f*)(gp4 + 4) - off;
        const u32 gu = pk((s[0] + s[2] + s8[0]) * rsd, (s[1] + s[3] + s8[1]) * rsd);
        const float gg = lo16(gu), uu = hi16(gu);
        const float hd = rbf(gg * __builtin_amdgcn_rcpf(1.f + ex2(-gg * LOG2E))) * uu;
        *(LAS unsigned short*)hwp = (unsigned short)pk(hd, hd);
      }
      bar();
      STAMP(14);
      FIN(); return;
    }
    if (isW) {
      const GAS char* op = (const GAS char*)wo + (size_t)((g * 2 + hh) * 128) * 1152 + cb3 * 384;
      LWAIT0(F_QK, 4);
#pragma unroll 1
      for (int gi = 0; gi < 12; ++gi) {
        WPUB(WINW - 4, N_V + gi * 4 - WINW + 4);
        const GAS char* s = op + ((gi & 3) * 36864 + (gi >> 2) * 128);
        const int d = L_T + (68 - 4 * gi) * 1024;
        DMA(s + o2, d, 0); DMA(s + ((o2 ^ 64) + 9216 - 1024), d, 1024); DMA(s + (o2 + 2 * 9216 - 2048), d, 2048); DMA(s + ((o2 ^ 64) + 3 * 9216 - 3072), d, 3072);
      }
      WPUB(0, N_W);
    } else {
      WPUB(24, N_V - 24); WPUB(16, N_V - 16); WPUB(8, N_V - 8); WPUB(0, N_V);
    }
    STAMP(11);
    if (!isR) { FIN(); return; }
    const u32 ep = *(LAS u32*)(L + L_EP);
    v4u tg = make_tag(ep);
    float xf = lo16(*(LAS unsigned short*)(L + L_X + r * 32 + (l & 3) * 8 + grp * 2));
    int go = (l >> 2) * 4608 + r * 64 + (l & 3) * 16;
    LAS char* frp = L + L_FR + (4 * (l & 3) + grp) * 48;
    GAS char* outp = (GAS char*)out + r * 32 + (l & 3) * 8 + grp * 2;
    PINV(tg); PINV(xf); PINV(go); PINV(frp); PINV(outp);
    if (isW) bar();
    float hv;
    {
      v4u p0, p1;
      GPOLL {
        CBAR();
        p0 = LDU(go, WS_RS) ^ tg; p1 = LDU(go, WS_RS + 16 * 4608) ^ tg;
        if (ALLOK(bad(p0) | bad(p1))) break;
      }
      STAMP(10);
      float s = tred4(asf4(p0) + asf4(p1));
      s += ror<4>(s); s += ror<8>(s);
      hv = rbf(xf + rbf(s));
      auto e = __builtin_amdgcn_permlane16_swap(asu(hv), asu(hv), false, false);
      auto e0 = __builtin_amdgcn_permlane32_swap(e[0], e[0], false, false);
      auto e1 = __builtin_amdgcn_permlane32_swap(e[1], e[1], false, false);
      STU((v4u{e0[0], e1[0], e0[1], e1[1]}) | tg, go, WS_BC);
      STAMP(9);
    }
    bar();
    bar();
    STAMP(14);
    {
      const v4f a0 = *(LAS v4f*)frp, a1 = *(LAS v4f*)(frp + 16); const float a2 = *(LAS float*)(frp + 32);
      const v4f a = a0 + a1;
      const float ov = hv + rbf(((a[0] + a[1]) + (a[2] + a[3])) + a2);
      if ((l & 12) == 0) *(GAS unsigned short*)outp = (unsigned short)pk(ov, ov);
      if (r == 0 && l == 0) __hip_atomic_store((GAS u32*)(ws + WS_EPOCH), ep + 1u, RLX);
    }
    STAMP(15);
    FIN(); return;
  }

  asm volatile("s_barrier" ::: "memory");
  __builtin_amdgcn_sched_barrier(0);
  const __amdgpu_buffer_rsrc_t rws = __builtin_amdgcn_make_buffer_rsrc(ws, 0, WS_END, 0x00020000);
  const int tq = (l >> 2) & 3, tp = (l & 3) * 8;
  u32 one = 1u, tmask = TMASK;
  PINV(fl); PINV(one); PINV(tmask);
  LWAIT(F_PROG, NX);
  v4u tg = make_tag(*(LAS u32*)(L + L_EP));
  PINV(tg);
  {
    const int rs = xcd >> 1, cbq = 2 * (t & 1) + (xcd & 1), gq_ = t >> 1;
    v4u xs[3];
    {
      LAS char* xp = L + L_X + (l >> 5) * 1152 + (l & 31) * 16;
      xs[0] = *(LAS v4u*)xp; xs[1] = *(LAS v4u*)(xp + 512);
      const v4u t2 = *(LAS v4u*)(xp + 1024 - ((l & 31) < 8 ? 0 : 1024));
      xs[2] = (l & 31) < 8 ? t2 : v4u{0u, 0u, 0u, 0u};
    }
    const v2u xa = *(LAS v2u*)(L + L_X + (l & 1) * 1152 + (rs * 144 + 16 * w + 4 * grp) * 2);
    float s = 0.f, ss = 0.f;
#pragma unroll
    for (int i = 0; i < 3; ++i)
#pragma unroll
      for (int k = 0; k < 4; ++k) { s = dot2(xs[i][k], 0x3F803F80u, s); ss = dot2(xs[i][k], xs[i][k], ss); }
    float mb, sd; ln_finish(s, ss, mb, sd);
    const float m0 = asf(__builtin_amdgcn_readlane(asu(mb), 0)), m1 = asf(__builtin_amdgcn_readlane(asu(mb), 32));
    const float d0 = asf(__builtin_amdgcn_readlane(asu(sd), 0)), d1 = asf(__builtin_amdgcn_readlane(asu(sd), 32));
    const float mm = (l & 1) ? m1 : m0, dd = __builtin_amdgcn_rcpf((l & 1) ? d1 : d0);
    v2u a;
#pragma unroll
    for (int k = 0; k < 2; ++k) { const u32 dlt = pk(lo16(xa[k]) - mm, hi16(xa[k]) - mm); a[k] = pk(lo16(dlt) * dd, hi16(dlt) * dd); }
    LAS char* qb = L + L_WQ + (2 * w + (grp >> 1)) * 1024 + (4 * (grp & 1) + tq) * 128 + tp;
    const int sw = ((tq >> 1) | (grp & 2)) << 5;
    LAS char* qb0 = qb + sw; LAS char* qb1 = qb + (32 ^ sw); LAS char* qb2 = qb + (64 ^ sw); LAS char* qb3 = qb + (96 ^ sw);
    LAS char* qw = L + L_QP + w * 512 + (l & 1) * 256 + (l & 6) * 32 + grp * 16;
    PINV(a); PINV(qb0); PINV(qb1); PINV(qb2); PINV(qb3); PINV(qw);
    LWAIT(F_PROG, 2 * w + 7);
    const v4f q0 = mfma16(trd(qb0), a, ZERO4), q1 = mfma16(trd(qb1), a, ZERO4), q2 = mfma16(trd(qb2), a, ZERO4), q3 = mfma16(trd(qb3), a, ZERO4);
    *(LAS v4f*)qw = sel4((l & 4) != 0, sel4((l & 2) != 0, q3, q2), sel4((l & 2) != 0, q1, q0));
    LINC(F_QP);
    STAMP(1);
    if (w == 8) {
      LAS char* qr = L + L_QP + (l & 31) * 16;
      int so = l < 32 ? grp * 4096 + (l & 15) * 16 : OOB, sb = WS_QG + gq_ * 8192 + rs * 1024 + cbq * 256;
      PINV(qr); PINV(so); PINS(sb);
      LWAIT0(F_QP, 9);
      const LAS v4f* q4p = (const LAS v4f*)qr;
      const v4f q4 = (((q4p[0] + q4p[32]) + (q4p[64] + q4p[96])) + ((q4p[128] + q4p[160]) + (q4p[192] + q4p[224]))) + q4p[256];
      const v4f q4r = asf4(__builtin_bit_cast(v4u, q4) + 0x200u);
      STU(ENC(q4r), so, sb);
      STAMP(2);
      int lo = l * 16, lb = WS_QG + g * 8192 + b * 4096;
      LAS char* qs = L + L_QS + l * 8;
      PINV(lo); PINS(lb); PINV(qs);
      LWAIT(F_PROG, QTRIG);
      v4u gq[4];
      GPOLL {
        CBAR();
#pragma unroll
        for (int i = 0; i < 4; ++i) gq[i] = LDU(lo + i * 1024, lb) ^ tg;
        if (ALLOK((bad(gq[0]) | bad(gq[1])) | (bad(gq[2]) | bad(gq[3])))) break;
      }
      const v4f qv = (asf4(gq[0]) + asf4(gq[1])) + (asf4(gq[2]) + asf4(gq[3]));
      *(LAS v2u*)qs = v2u{pk(qv[0], qv[1]), pk(qv[2], qv[3])};
      LINC(F_Q);
      STAMP(3);
      LWAIT(F_P, 4);
      const v4f ma = *(LAS v4f*)(L + L_WS + (l >> 5) * 32), la = *(LAS v4f*)(L + L_WS + (l >> 5) * 32 + 16);
      const float mc = fmaxf(fmaxf(ma[0], ma[1]), fmaxf(ma[2], ma[3]));
      const float lt = (la[0] * ex2((ma[0] - mc) * LOG2E) + la[1] * ex2((ma[1] - mc) * LOG2E)) + (la[2] * ex2((ma[2] - mc) * LOG2E) + la[3] * ex2((ma[3] - mc) * LOG2E));
      const v4f sq = {mc, lt, asf(__builtin_amdgcn_readlane(asu(mc), 32)), asf(__builtin_amdgcn_readlane(asu(lt), 32))};
      STU(ENC(sq), l == 0 ? 0 : OOB, WS_ST + g * 256 + u * 16);
      STAMP(5);
    }
  }
  if (w >= 4 && w < 8) {
    const int v = w - 4;
    const int kx = (l & 15) * 256 + ((grp ^ (l & 15)) << 4);
    const int ka = L_T + ((2 * v + rot8) & 7) * 8192 + kx, kb = L_T + ((2 * v + 1 + rot8) & 7) * 8192 + kx;
    LAS char* qa = L + L_QS + (l & 1) * 256 + grp * 16;
    LAS char* pw = L + L_P + (l >> 5) * 512 + v * 128 + (l & 31) * 2;
    LAS char* sp = L + L_WS + (l >> 5) * 32 + v * 4;
    PINV(qa); PINV(pw); PINV(sp);
    LWAIT(F_PROG, N_K);
    v4u kf[4][4];
#pragma unroll
    for (int ds = 0; ds < 4; ++ds) {
      kf[ds][0] = *(LAS v4u*)(L + (ka ^ (ds << 6))); kf[ds][1] = *(LAS v4u*)(L + (ka ^ (ds << 6)) + 4096);
      kf[ds][2] = *(LAS v4u*)(L + (kb ^ (ds << 6))); kf[ds][3] = *(LAS v4u*)(L + (kb ^ (ds << 6)) + 4096);
    }
#pragma unroll
    for (int ds = 0; ds < 4; ++ds) { PINV(kf[ds][0]); PINV(kf[ds][1]); PINV(kf[ds][2]); PINV(kf[ds][3]); }
    LINC(F_QK);
    LWAIT0(F_Q, 1);
    v4u qf[4];
#pragma unroll
    for (int ds = 0; ds < 4; ++ds) qf[ds] = *(LAS v4u*)(qa + ds * 64);
    v4f s[4] = {ZERO4, ZERO4, ZERO4, ZERO4};
#pragma unroll
    for (int ds = 0; ds < 4; ++ds)
#pragma unroll
      for (int kt = 0; kt < 4; ++kt) s[kt] = mfma(qf[ds], kf[ds][kt], s[kt]);
    const bool odd = (grp & 1) != 0, up = l >= 32;
    const float a0 = odd ? s[1][0] : s[0][0], a1 = odd ? s[1][1] : s[0][1], b0 = odd ? s[3][0] : s[2][0], b1 = odd ? s[3][1] : s[2][1];
    const u32 r1 = pk(up ? a1 : a0, up ? b1 : b0);
    const u32 r2 = pk(lo16(r1) * 0.08838834764831845f, hi16(r1) * 0.08838834764831845f);
    const float fa = lo16(r2), fb = hi16(r2);
    const float mx = max16x(rowmax(fmaxf(fa, fb)));
    const float pa = ex2((fa - mx) * LOG2E), pb = ex2((fb - mx) * LOG2E);
    const u32 pp = pk(pa, pb);
    *(LAS unsigned short*)pw = (unsigned short)pp;
    *(LAS unsigned short*)(pw + 64) = (unsigned short)(pp >> 16);
    float ls = rowsum(pa + pb); ls = t16(ls, ls);
    *(LAS float*)sp = mx; *(LAS float*)(sp + 16) = ls;
    LINC(F_P);
    STAMP(5);
  }
  if (w < 4) {
    const int vrow = (8 * grp + tq) * 256 + (l & 1) * 8;
    const int c0 = (4 * w + ((l >> 1) & 1)) ^ (tq << 2) ^ ((2 * grp) & 3);
    LAS char* va00 = L + L_V + vrow + (c0 << 4); LAS char* va01 = L + L_V + vrow + 1024 + ((c0 ^ 1) << 4);
    LAS char* va10 = L + L_V + vrow + ((c0 ^ 2) << 4); LAS char* va11 = L + L_V + vrow + 1024 + ((c0 ^ 3) << 4);
    LAS char* pa = L + L_P + (l & 1) * 512 + grp * 16;
    LAS char* mp = L + L_WS + (l & 1) * 32;
    int so = (l & 12) == 0 ? (l & 1) * 8192 + (l & 2) * 32 + grp * 16 : OOB, sb = WS_OG + g * 16384 + u * 512 + w * 128;
    PINV(va00); PINV(va01); PINV(va10); PINV(va11); PINV(pa); PINV(mp); PINV(so); PINS(sb);
    v4u va[8], vb[8];
#define VREAD(i) do { va[i] = cat(trd(va00 + (i) * 8192), trd(va01 + (i) * 8192)); vb[i] = cat(trd(va10 + (i) * 8192), trd(va11 + (i) * 8192)); } while (0)
    LWAIT(F_PROG, N_K + 32);
    VREAD(0); VREAD(1); VREAD(2); VREAD(3);
    LWAIT0(F_P, 4);
    STAMP(5);
    v4u pf[8];
#pragma unroll
    for (int i = 0; i < 8; ++i) pf[i] = *(LAS v4u*)(pa + i * 64);
    const v4f mg = *(LAS v4f*)mp;
    const float mc = fmaxf(fmaxf(mg[0], mg[1]), fmaxf(mg[2], mg[3]));
    float f0 = ex2((mg[0] - mc) * LOG2E), f1 = ex2((mg[1] - mc) * LOG2E), f2 = ex2((mg[2] - mc) * LOG2E), f3 = ex2((mg[3] - mc) * LOG2E);
    const bool n1 = (l & 2) != 0;
    v4f o = sel4(n1, mfma(vb[1], pf[1], mfma(vb[0], pf[0], ZERO4)), mfma(va[1], pf[1], mfma(va[0], pf[0], ZERO4))) * f0;
    o += sel4(n1, mfma(vb[3], pf[3], mfma(vb[2], pf[2], ZERO4)), mfma(va[3], pf[3], mfma(va[2], pf[2], ZERO4))) * f1;
    PINV(o); PINV(f2); PINV(f3);
    LWAIT0(F_PROG, N_K + 48);
    VREAD(4); VREAD(5);
    o += sel4(n1, mfma(vb[5], pf[5], mfma(vb[4], pf[4], ZERO4)), mfma(va[5], pf[5], mfma(va[4], pf[4], ZERO4))) * f2;
    PINV(o);
    LWAIT0(F_PROG, N_K + 56);
    VREAD(6);
    v4f ta = mfma(va[6], pf[6], ZERO4), tb = mfma(vb[6], pf[6], ZERO4);
    LWAIT0(F_PROG, N_V);
    VREAD(7);
    o += sel4(n1, mfma(vb[7], pf[7], tb), mfma(va[7], pf[7], ta)) * f3;
    STU(ENC(o), so, sb);
    LINC(F_PV);
    STAMP(7);
  }
  const int t8 = tq * 128 + tp, sw8 = ((tq >> 1) | ((grp & 1) << 1)) << 5;
  if (isW) {
    const int hq = g * 2 + hh;
    if (w >= 4 && w < 8) {
      LWAIT(F_PROG, STRIG);
      v4u s0, s1;
      GPOLL {
        CBAR();
        s0 = LDU((l >> 3) * 16, WS_ST + g * 256) ^ tg; s1 = LDU((l >> 3) * 16 + 128, WS_ST + g * 256) ^ tg;
        if (ALLOK(bad(s0) | bad(s1))) break;
      }
      float wn[2];
#pragma unroll
      for (int i = 0; i < 2; ++i) {
        const v4u st = i ? s1 : s0;
        const float mj = asf(hh ? st[2] : st[0]), lj = asf(hh ? st[3] : st[1]);
        float M = fmaxf(mj, ror<8>(mj)); M = max32x(max16x(M));
        const float e = ex2((mj - M) * LOG2E);
        float Ls = e * lj; Ls += ror<8>(Ls); Ls = t16(Ls, Ls); Ls = t32(Ls, Ls);
        wn[i] = e * __builtin_amdgcn_rcpf(Ls);
      }
      int lo = (l >> 3) * 512 + (l & 7) * 16, lb = WS_OG + hq * 8192 + (w - 4) * 128;
      LAS char* ob = L + L_OB + ((l >> 3) & 1) * 256 + (w - 4) * 64 + (l & 7) * 8 + grp * 2;
      PINV(wn[0]); PINV(wn[1]); PINV(lo); PINS(lb); PINV(ob);
      LWAIT0(F_PV, 4);
      v4u o0, o1;
      GPOLL {
        CBAR();
        o0 = LDU(lo, lb) ^ tg; o1 = LDU(lo, lb + 4096) ^ tg;
        if (ALLOK(bad(o0) | bad(o1))) break;
      }
      STAMP(8);
      const float r0 = tred4(asf4(o0) * wn[0]), r1 = tred4(asf4(o1) * wn[1]);
      const bool hi8 = (l & 8) != 0;
      const float val = (hi8 ? r1 : r0) + ror<8>(hi8 ? r0 : r1);
      *(LAS unsigned short*)ob = (unsigned short)pk(val, val);
      bar();
    } else if (w < 4) {
      LAS char* of = L + L_OB + (l & 1) * 256 + grp * 16;
      int so = (l & 15) < 6 ? (l & 1) * 2304 + (l & 6) * 32 + grp * 16 : OOB, sb = WS_RS + hq * 4608 + cb3 * 768 + w * 192;
      PINV(of); PINV(so); PINS(sb);
      LWAIT(F_PROG, N_W);
      v4u wf[3][4];
#pragma unroll
      for (int n = 0; n < 3; ++n) {
        LAS char* wp = L + L_T + (56 - 16 * ((3 * w + n) >> 2) + grp) * 1024 + t8 + ((((3 * w + n) & 3) << 5) ^ sw8);
#pragma unroll
        for (int ks = 0; ks < 4; ++ks) { wf[n][ks] = trd2(wp + (3 - ks) * 4096, 512); PINV(wf[n][ks]); }
      }
      bar();
      v4f y[3] = {ZERO4, ZERO4, ZERO4};
      v4u oq[4];
#pragma unroll
      for (int ks = 0; ks < 4; ++ks) oq[ks] = *(LAS v4u*)(of + ks * 64);
#pragma unroll
      for (int ks = 0; ks < 4; ++ks)
#pragma unroll
        for (int n = 0; n < 3; ++n) y[n] = mfma(wf[n][ks], oq[ks], y[n]);
      const v4f yv = sel4((l & 4) != 0, y[2], sel4((l & 2) != 0, y[1], y[0]));
      STU(ENC(yv), so, sb);
      STAMP(9);
    } else {
      bar();
    }
  }
  if (!isM) {
    if (isR) {
      int go = (l >> 2) * 4608 + r * 64 + (l & 3) * 16, gb = WS_H4 + w * 16 * 4608;
      LAS char* fr = L + L_FR + (4 * (l & 3) + grp) * 48 + w * 4;
      PINV(go); PINS(gb); PINV(fr);
      bar();
      v4u p;
      GPOLL {
        CBAR();
        p = LDU(go, gb) ^ tg;
        if (ALLOK(bad(p))) break;
      }
      STAMP(12);
      float s = tred4(asf4(p));
      s += ror<4>(s); s += ror<8>(s);
      *(LAS float*)fr = s;
      bar();
    }
    FIN(); return;
  }
  {
    LWAIT(F_PROG, N_T3);
    STAMP(11);
    v2u gf[4][2][2];
#pragma unroll
    for (int kb = 0; kb < 4; ++kb) {
      const int rg = 4 * w + kb;
      const int base = (rg < 18 ? L_T + (71 - 2 * rg) * 1024 : L_V + (2 * rg - 36) * 1024) + (4 * grp + tq) * 64 + tp, ms = rg < 18 ? -1024 : 1024;
#pragma unroll
      for (int mat = 0; mat < 2; ++mat)
#pragma unroll
        for (int nt = 0; nt < 2; ++nt) gf[kb][mat][nt] = trd(L + base + mat * ms + ((nt ^ (grp >> 1)) << 5));
    }
    const bool n1 = l >= 32;
    const int grow = (grp & 1) * 32 + (grp >> 1) * 16 + (l & 15);
    {
      const v2u ones = {0x3F803F80u, 0x3F803F80u};
      v4f cs[2][2];
#pragma unroll
      for (int mat = 0; mat < 2; ++mat)
#pragma unroll
        for (int nt = 0; nt < 2; ++nt) cs[mat][nt] = mfma16(ones, gf[3][mat][nt], mfma16(ones, gf[2][mat][nt], mfma16(ones, gf[1][mat][nt], mfma16(ones, gf[0][mat][nt], ZERO4))));
      if ((grp & 1) == 0) *(LAS v2f*)(L + L_SP + grow * 80 + w * 8) = v2f{n1 ? cs[0][1][0] : cs[0][0][0], n1 ? cs[1][1][0] : cs[1][0][0]};
    }
    int ho = ((l >> 2) & 1) * 2304 + (64 * w + 16 * (l & 3) + 4 * grp) * 4, bo = WS_BC + (xcd * 2 + (t & 1)) * BC_STRIDE;
    LAS char* gw = L + L_GP + grow * 80 + w * 8;
    LAS char* hr = L + L_HW + (l & 1) * 64 + grp * 16;
    int so = (l & 8) == 0 ? (l & 1) * 2304 + (l & 6) * 32 + grp * 16 : OOB, sb = WS_H4 + m * 4608 + w * 256;
    PINV(ho); PINS(bo); PINV(gw); PINV(hr); PINV(so); PINS(sb);
    bar();
    v4u df[4];
#pragma unroll
    for (int nt = 0; nt < 4; ++nt) { df[nt] = trd2(L + L_T + (4 * w + grp) * 1024 + t8 + ((nt << 5) ^ sw8), 512); }
    v4u hu;
    GPOLL {
      CBAR();
      hu = LDU(ho, bo);
      if (ALLOK(bad(hu ^ tg))) break;
    }
    STAMP(12);
    const v2u ha = {__builtin_amdgcn_perm(hu[1], hu[0], 0x07060302u), __builtin_amdgcn_perm(hu[3], hu[2], 0x07060302u)};
    float y[2][2];
#pragma unroll
    for (int mat = 0; mat < 2; ++mat)
#pragma unroll
      for (int nt = 0; nt < 2; ++nt) {
        const v4f a0 = mfma16(ha, gf[0][mat][nt], ZERO4), a1 = mfma16(ha, gf[1][mat][nt], ZERO4), a2 = mfma16(ha, gf[2][mat][nt], ZERO4), a3 = mfma16(ha, gf[3][mat][nt], ZERO4);
        y[mat][nt] = (a0[0] + a1[1]) + (a2[2] + a3[3]);
      }
    *(LAS v2f*)gw = v2f{n1 ? y[0][1] : y[0][0], n1 ? y[1][1] : y[1][0]};
    bar();
    STAMP(13);
    bar();
    const v4u da = *(LAS v4u*)hr;
    v4f d[4];
#pragma unroll
    for (int nt = 0; nt < 4; ++nt) d[nt] = mfma(df[nt], da, ZERO4);
    const v4f dv = sel4((l & 4) != 0, sel4((l & 2) != 0, d[3], d[2]), sel4((l & 2) != 0, d[1], d[0]));
    STU(ENC(dv), so, sb);
    STAMP(14);
  }
  FIN();
}

__global__ void dbg_clear(u64* p) { for (int i = threadIdx.x + blockIdx.x * blockDim.x; i < 256 * 16 * 16; i += gridDim.x * blockDim.x) p[i] = 0; }
__global__ void dbg_print(const u64* p) {
  if (threadIdx.x) return;
  u64 t0 = ~0ull, t1 = 0;
  for (int i = 0; i < 256 * 16 * 16; ++i) if (p[i]) { t0 = p[i] < t0 ? p[i] : t0; t1 = p[i] > t1 ? p[i] : t1; }
  printf("span %d\n", (int)(t1 - t0));
  for (int w = 0; w < 10; ++w) { if (w != 0 && w != 4 && w < 8) continue;
    for (int s = 0; s < 16; ++s) { u64 mn = ~0ull, mx = 0, sum = 0; int n = 0;
      for (int c = 0; c < 256; ++c) { u64 v = p[(c * 16 + w) * 16 + s]; if (v) { mn = v < mn ? v : mn; mx = v > mx ? v : mx; sum += v - t0; ++n; } }
      if (n) printf("w%d s%d: %d / %d / %d\n", w, s, (int)(mn - t0), (int)(sum / n), (int)(mx - t0)); } }
}
extern "C" void kernel_launch(void* const* d_in, const int* in_sizes, int n_in, void* d_out, int out_size, void* d_ws, size_t ws_size, hipStream_t stream) {
  (void)hipFuncSetAttribute((const void*)fused_decode, hipFuncAttributeMaxDynamicSharedMemorySize, L_END);
  for (int i = 0; i < 9; ++i) fprintf(stderr, "in%d %p\n", i, d_in[i]);
  fprintf(stderr, "out %p ws %p\n", d_out, d_ws);
  dbg_clear<<<64, 256, 0, stream>>>((u64*)((char*)d_ws + STAMP_OFF));
  fused_decode<<<256, 640, L_END, stream>>>((const char*)d_in[0], (char*)d_ws, (const char*)d_in[3], (const char*)d_in[1], (const char*)d_in[2], (const char*)d_in[4],
                                           (const char*)d_in[6], (const char*)d_in[7], (const char*)d_in[8], (char*)d_out);
  dbg_print<<<1, 64, 0, stream>>>((const u64*)((char*)d_ws + STAMP_OFF));
}
```

```cpp
#define STAMPS
#include <hip/hip_runtime.h>
#include <stdint.h>

#define GAS __attribute__((address_space(1)))
#define LAS __attribute__((address_space(3)))
typedef unsigned u32;
typedef unsigned long long u64;
typedef unsigned v4u __attribute__((ext_vector_type(4)));
typedef unsigned v2u __attribute__((ext_vector_type(2)));
typedef float v4f __attribute__((ext_vector_type(4)));
typedef float v2f __attribute__((ext_vector_type(2)));
typedef __bf16 v8bf __attribute__((ext_vector_type(8)));
typedef __bf16 v2bf __attribute__((ext_vector_type(2)));
typedef short v4s __attribute__((ext_vector_type(4)));

#define WIN 28
#define WINW 28
#define QTRIG 64
#define STRIG 140
#define PFMODE 0
#define CODE_KB 14

constexpr int WS_EPOCH = 0, WS_ST = 4096, WS_QG = 8192, WS_OG = WS_QG + 131072, WS_RS = WS_OG + 262144, WS_BC = WS_RS + 32 * 4608, BC_STRIDE = 4608,
              WS_H4 = WS_BC + 16 * BC_STRIDE, WS_END = WS_H4 + 144 * 4608;
constexpr int L_T = 0, L_WQ = 65536, L_GP = 73728, L_SP = L_GP + 5120, L_X = 83968, L_JUNK = 87040, L_V = 88064, L_FLAG = L_V + 65536, L_EP = L_FLAG + 64,
              L_QP = L_EP + 256, L_QS = L_QP + 4608, L_P = L_QS + 512, L_WS = L_P + 1024, L_OB = L_WS + 64, L_HW = L_OB + 512, L_FR = L_HW + 128, L_END = L_FR + 768;
static_assert(L_END <= 163840 && L_SP + 2560 <= L_X && L_WQ + 18432 <= L_X, "LDS");
constexpr int F_PROG = 0, F_QP = 1, F_Q = 2, F_QK = 3, F_P = 4, F_PV = 5;
constexpr int NX = 4, N0 = 24, N_K = N0 + 64, N_V = N_K + 64, N_W = N_V + 48, N_T3 = 400;
constexpr int SC1 = 16, NT = 2;
constexpr u32 GSPIN = 1u << 17, LSPIN = 1u << 22;
constexpr int OOB = 0x7ffffff0;
constexpr u32 TMASK = 0x3FFu;

constexpr size_t STAMP_OFF = 128u << 20;
static const char* stamp_name(int s) { static const char* n[16] = {"start", "qpart", "qpub", "qrdy", "qk", "p", "Vland", "opub", "ordy", "hpub", "rs", "t3", "hrdy", "barB", "dpub", "end"}; return n[s]; }
#define STAMP(i) ts[i] = __builtin_amdgcn_s_memrealtime()
#define FIN() do { if (l == 0) { u64* sp_ = (u64*)(ws + STAMP_OFF) + ((size_t)c * 16 + (wid == 0 ? 9 : wid - 1)) * 16; _Pragma("unroll") for (int i_ = 0; i_ < 16; ++i_) sp_[i_] = ts[i_]; } } while (0)

#define CBAR() asm volatile("" ::: "memory")
#define PINV(x) asm volatile("" : "+v"(x))
#define PINS(x) asm volatile("" : "+s"(x))
#define GPOLL _Pragma("nounroll") for (u32 sp_ = 0; sp_ < GSPIN; ++sp_)
#define RLX __ATOMIC_RELAXED, __HIP_MEMORY_SCOPE_AGENT
__device__ __forceinline__ void bar() { asm volatile("s_waitcnt lgkmcnt(0)\n\ts_barrier" ::: "memory"); __builtin_amdgcn_sched_barrier(0); }
__device__ __forceinline__ u32 pk(float lo, float hi) { return __builtin_bit_cast(u32, __builtin_convertvector(v2f{lo, hi}, v2bf)); }
__device__ __forceinline__ float asf(u32 u) { return __builtin_bit_cast(float, u); }
__device__ __forceinline__ u32 asu(float f) { return __builtin_bit_cast(u32, f); }
__device__ __forceinline__ v4f asf4(v4u u) { return __builtin_bit_cast(v4f, u); }
__device__ __forceinline__ float lo16(u32 u) { return asf(u << 16); }
__device__ __forceinline__ float hi16(u32 u) { return asf(u & 0xffff0000u); }
__device__ __forceinline__ float rbf(float f) { return lo16(pk(f, f)); }
__device__ __forceinline__ v4f mfma(v4u a, v4u b, v4f c) { return __builtin_amdgcn_mfma_f32_16x16x32_bf16(__builtin_bit_cast(v8bf, a), __builtin_bit_cast(v8bf, b), c, 0, 0, 0); }
__device__ __forceinline__ v4f mfma16(v2u a, v2u b, v4f c) { return __builtin_amdgcn_mfma_f32_16x16x16bf16_1k(__builtin_bit_cast(v4s, a), __builtin_bit_cast(v4s, b), c, 0, 0, 0); }
__device__ __forceinline__ float dot2(u32 a, u32 b, float c) { return __builtin_amdgcn_fdot2_f32_bf16(__builtin_bit_cast(v2bf, a), __builtin_bit_cast(v2bf, b), c, false); }
template <int N> __device__ __forceinline__ float ror(float x) { return asf(__builtin_amdgcn_mov_dpp(asu(x), 0x120 + N, 0xf, 0xf, true)); }
__device__ __forceinline__ float t16(float x, float y) { auto s = __builtin_amdgcn_permlane16_swap(asu(x), asu(y), false, false); return asf(s[0]) + asf(s[1]); }
__device__ __forceinline__ float t32(float x, float y) { auto s = __builtin_amdgcn_permlane32_swap(asu(x), asu(y), false, false); return asf(s[0]) + asf(s[1]); }
__device__ __forceinline__ float tred4(v4f v) { return t16(t32(v[0], v[2]), t32(v[1], v[3])); }
__device__ __forceinline__ float max16x(float x) { auto s = __builtin_amdgcn_permlane16_swap(asu(x), asu(x), false, false); return fmaxf(asf(s[0]), asf(s[1])); }
__device__ __forceinline__ float max32x(float x) { auto s = __builtin_amdgcn_permlane32_swap(asu(x), asu(x), false, false); return fmaxf(asf(s[0]), asf(s[1])); }
__device__ __forceinline__ float rowsum(float x) { x += ror<8>(x); x += ror<4>(x); x += ror<2>(x); x += ror<1>(x); return x; }
__device__ __forceinline__ float rowmax(float x) { x = fmaxf(x, ror<8>(x)); x = fmaxf(x, ror<4>(x)); x = fmaxf(x, ror<2>(x)); x = fmaxf(x, ror<1>(x)); return x; }
__device__ __forceinline__ float ex2(float x) { return __builtin_amdgcn_exp2f(x); }
__device__ __forceinline__ v2u trd(LAS char* p) { return __builtin_bit_cast(v2u, __builtin_amdgcn_ds_read_tr16_b64_v4i16((LAS v4s*)p)); }
__device__ __forceinline__ v4u cat(v2u a, v2u b) { return v4u{a[0], a[1], b[0], b[1]}; }
__device__ __forceinline__ v4u trd2(LAS char* p, int d) { return cat(trd(p), trd(p + d)); }
__device__ __forceinline__ v4f sel4(bool c, v4f a, v4f b) { return v4f{c ? a[0] : b[0], c ? a[1] : b[1], c ? a[2] : b[2], c ? a[3] : b[3]}; }
__device__ __forceinline__ u32 bfi(u32 mask, u32 a, u32 b) { u32 r; asm("v_bfi_b32 %0, %1, %2, %3" : "=v"(r) : "v"(mask), "v"(a), "v"(b)); return r; }
#define ENC(v) (v4u{bfi(tmask, tg[0], asu((v)[0])), bfi(tmask, tg[1], asu((v)[1])), bfi(tmask, tg[2], asu((v)[2])), bfi(tmask, tg[3], asu((v)[3]))})
__device__ __forceinline__ u32 bad(v4u y) { return (y[0] | y[1]) | (y[2] | y[3]); }
#define ALLOK(x) (__builtin_amdgcn_ballot_w64(((x) & TMASK) != 0u) == 0ull)
constexpr float LOG2E = 1.4426950408889634f;
#define ZERO4 (v4f{0.f, 0.f, 0.f, 0.f})

__device__ __forceinline__ void ln_finish(float s, float ss, float& mean_bf, float& sd_bf) {
  float z = rowsum(t16(s, ss));
  auto e = __builtin_amdgcn_permlane16_swap(asu(z), asu(z), false, false);
  const float m = asf(e[0]) * (1.f / 576.f);
  const float var = fmaxf(asf(e[1]) * (1.f / 576.f) - m * m, 0.f);
  mean_bf = rbf(m);
  sd_bf = rbf(__builtin_amdgcn_sqrtf(rbf(rbf(var) + 1e-5f)));
}
#define WPUB(A, val) asm volatile("s_waitcnt vmcnt(%0)\n\tds_write_b32 %1, %2" :: "i"(A), "v"(fl), "v"(val) : "memory")
template <int F, int SLEEP> __device__ __forceinline__ void lwait(u32 fl, u32 n) {
#pragma nounroll
  for (u32 sp = 0; sp < LSPIN; ++sp) {
    u32 v; asm volatile("ds_read_b32 %0, %1 offset:%2\n\ts_waitcnt lgkmcnt(0)" : "=v"(v) : "v"(fl), "i"(4 * F) : "memory");
    if ((u32)__builtin_amdgcn_readfirstlane(v) >= n) break;
    if (SLEEP) __builtin_amdgcn_s_sleep(SLEEP);
  }
  __builtin_amdgcn_sched_barrier(0);
}
#define LWAIT(F, n) lwait<F, 1>(fl, (u32)(n))
#define LWAIT0(F, n) lwait<F, 0>(fl, (u32)(n))
#define LINC(F) asm volatile("s_mov_b64 exec, 1\n\tds_add_u32 %0, %1 offset:%2\n\ts_mov_b64 exec, -1" :: "v"(fl), "v"(one), "i"(4 * (F)) : "memory")
#define DMA0(src, dst, imm) __builtin_amdgcn_global_load_lds((const GAS u32*)(src), (LAS u32*)(L + (dst)), 16, imm, 0)
#define DMA(src, dst, imm) __builtin_amdgcn_global_load_lds((const GAS u32*)(src), (LAS u32*)(L + (dst)), 16, imm, NT)
#define LDU(off, soff) __builtin_amdgcn_raw_buffer_load_b128(rws, (off), (soff), SC1)
#define STU(v, off, soff) __builtin_amdgcn_raw_buffer_store_b128((v), rws, (off), (soff), SC1)
__device__ __forceinline__ v4u make_tag(u32 ep) { const u32 t = (ep + 1u) * 0x2F5u; return v4u{t & TMASK, (t + 341u) & TMASK, (t + 682u) & TMASK, (t + 1023u) & TMASK}; }

__global__ void __launch_bounds__(640) fused_decode(const char* __restrict__ x, char* __restrict__ ws, const char* __restrict__ wq, const char* __restrict__ kc,
                                                    const char* __restrict__ vc, const char* __restrict__ wo, const char* __restrict__ w1,
                                                    const char* __restrict__ w3, const char* __restrict__ w2, char* __restrict__ out) {
  extern __shared__ __attribute__((aligned(16))) char lds_[];
  LAS char* L = (LAS char*)lds_;
  u64 pc0; asm volatile("s_getpc_b64 %0" : "=s"(pc0));
  const int l = threadIdx.x & 63, wid = __builtin_amdgcn_readfirstlane(threadIdx.x >> 6), w = wid - 1;
  const int c = blockIdx.x, xcd = c & 7, t = c >> 3, g = xcd * 2 + (t >> 4), u = t & 15, b = u >> 3, j = u & 7;
  const int grp = l >> 4;
  const bool isM = u < 9, isW = u >= 9 && u < 15;
  const int m = g * 9 + u, r = g * 7 + (u - 9), hh = u >= 12 ? 1 : 0, cb3 = u - 9 - 3 * hh;
  const bool isR = !isM && r < 72;
  const int rot = (c + (c >> 4)) & 15, rot8 = rot >> 1;
  u32 fl = L_FLAG;
  u64 ts[16];
#pragma unroll
  for (int i = 0; i < 16; ++i) ts[i] = 0;
  STAMP(0);

  if (__builtin_expect(wid == 0, 1)) {
    __builtin_amdgcn_s_setprio(3);
    const u32 p8r = l >> 3, p8c = ((l & 7) ^ ((l >> 3) & 2)) << 4;
    {
      DMA0((const GAS char*)x + l * 16, L_X, 0); DMA0((const GAS char*)x + l * 16, L_X, 1024); DMA0((const GAS char*)x + (l & 15) * 16, L_X, 2048);
      __builtin_amdgcn_global_load_lds((const GAS u32*)(ws + WS_EPOCH), (LAS u32*)(L + L_EP), 4, 0, SC1);
      DMA0((pc0 & ~255ull) + (u < CODE_KB ? u : CODE_KB - 1) * 1024 + l * 16, L_JUNK, 0);
      const GAS char* qp = (const GAS char*)wq + (size_t)((xcd >> 1) * 144) * 8192 + t * 256 + (xcd & 1) * 128;
      const u32 o = p8r * 8192 + p8c, o1 = (o ^ 64) + 65536 - 1024;
#pragma unroll 1
      for (int i = 0; i < 9; ++i) { const GAS char* s_ = qp + i * 131072; DMA(s_ + o, L_WQ + i * 2048, 0); DMA(s_ + o1, L_WQ + i * 2048, 1024); }
      __builtin_amdgcn_global_load_lds((const GAS u32*)x, (LAS u32*)(L + L_JUNK), 4, 0, 0);
    }
    const size_t reg = ((size_t)(b * 16 + g) * 8192 + 6144 + j * 256) * 256;
    const GAS char* kp = (const GAS char*)kc + reg;
    const u32 kv0 = grp * 256 + (((l & 15) ^ grp) << 4);
#define KGRP(gi) do { const u32 go_ = (((gi) + rot) & 15) * 4096; const GAS char* s_ = kp + go_; \
      DMA(s_ + kv0, L_T + go_, 0); DMA(s_ + (kv0 ^ 64), L_T + go_, 1024); DMA(s_ + (kv0 ^ 128), L_T + go_, 2048); DMA(s_ + (kv0 ^ 192), L_T + go_, 3072); } while (0)
    KGRP(0);
    if (l < 16) asm volatile("ds_write_b32 %0, %1" :: "v"(L_FLAG + l * 4), "v"(0) : "memory");
    bar();
    WPUB(N0 + 4 - NX, NX);
    STAMP(3);
#define PACE(n) do { if (WINW != WIN && isW) WPUB(WINW - 4, (n) - WINW + 4 > NX ? (n) - WINW + 4 : NX); else WPUB(WIN - 4, (n) - WIN + 4 > NX ? (n) - WIN + 4 : NX); } while (0)
#pragma unroll 1
    for (int gi = 1; gi < 16; ++gi) { PACE(N0 + gi * 4); KGRP(gi); }
    STAMP(4);
    const GAS char* vp = (const GAS char*)vc + reg;
    const u32 vv0 = grp * 256 + (((l & 15) ^ (grp << 2)) << 4);
#pragma unroll 1
    for (int gi = 0; gi < 16; ++gi) {
      PACE(N_K + gi * 4);
      const GAS char* s = vp + (((gi + 2 * rot8) & 15) ^ (rot & 1)) * 4096; const int d = L_V + (gi ^ (rot & 1)) * 4096;
      DMA(s + vv0, d, 0); DMA(s + (vv0 ^ 16), d, 1024); DMA(s + (vv0 ^ 32), d, 2048); DMA(s + (vv0 ^ 48), d, 3072);
    }
    STAMP(5);
    const __amdgpu_buffer_rsrc_t rws = __builtin_amdgcn_make_buffer_rsrc(ws, 0, WS_END, 0x00020000);
    const u32 o2 = p8r * 1152 + p8c;
    LWAIT0(F_QP, 9);
    if (isM) {
      const u32 o13 = (l >> 2) * 9216 + (((l & 3) ^ ((l >> 4) & 2)) << 4) + m * 64;
#define STRIP(rg, dst) do { DMA((const GAS char*)w1 + (size_t)(rg) * 147456 + o13, (dst), 0); DMA((const GAS char*)w3 + (size_t)(rg) * 147456 + o13, (dst) + ((rg) < 18 ? -1024 : 1024), 0); } while (0)
      LWAIT0(F_QK, 4);
      WPUB(24, N_V - 24); WPUB(16, N_V - 16); STRIP(0, L_T + 71 * 1024); STRIP(1, L_T + 69 * 1024);
      WPUB(12, N_V - 8); STRIP(2, L_T + 67 * 1024); STRIP(3, L_T + 65 * 1024);
      WPUB(8, N_V);
      STAMP(6);
      STRIP(4, L_T + 63 * 1024); STRIP(5, L_T + 61 * 1024);
      LWAIT0(F_PV, 4);
      constexpr int RG0 = 6;
#pragma unroll 1
      for (int rg = RG0; rg < 18; ++rg) { asm volatile("s_waitcnt vmcnt(56)" ::: "memory"); STRIP(rg, L_T + (71 - 2 * rg) * 1024); }
#pragma unroll 1
      for (int rg = 18; rg < 36; ++rg) { asm volatile("s_waitcnt vmcnt(56)" ::: "memory"); STRIP(rg, L_V + (2 * rg - 36) * 1024); }
      const GAS char* dp = (const GAS char*)w2 + (size_t)m * 36864;
#pragma unroll 1
      for (int i = 0; i < 9; ++i) {
        asm volatile("s_waitcnt vmcnt(56)" ::: "memory");
        const GAS char* s = dp + i * 128;
        DMA(s + o2, L_T + i * 4096, 0); DMA(s + ((o2 ^ 64) + 9216 - 1024), L_T + i * 4096, 1024);
        DMA(s + (o2 + 2 * 9216 - 2048), L_T + i * 4096, 2048); DMA(s + ((o2 ^ 64) + 3 * 9216 - 3072), L_T + i * 4096, 3072);
      }
      WPUB(36, N_T3);
      STAMP(10);
      asm volatile("s_waitcnt vmcnt(0)" ::: "memory");
      STAMP(11);
      v4u tg = make_tag(*(LAS u32*)(L + L_EP));
      int xso = (l >> 5) * 2304 + (l & 31) * 16, xso4 = (l >> 5) * 2304 + (l & 15) * 16 + 2048, bo = WS_BC + (xcd * 2 + (t & 1)) * BC_STRIDE;
      LAS char* gpp = L + L_GP + l * 80; LAS char* hwp = L + L_HW + l * 2;
      bar();
      v2f cs2;
      {
        const LAS v4f* sp = (const LAS v4f*)(L + L_SP + (l & 31) * 80);
        const v4f s = (sp[0] + sp[1]) + (sp[2] + sp[3]);
        const v2f s8 = *(const LAS v2f*)(sp + 4);
        cs2 = v2f{s[0] + s[2] + s8[0], s[1] + s[3] + s8[1]};
      }
      PINV(cs2); PINV(tg); PINV(xso); PINV(xso4); PINS(bo); PINV(gpp); PINV(hwp);
      v4u hx[5];
      GPOLL {
        CBAR();
#pragma unroll
        for (int i = 0; i < 4; ++i) hx[i] = LDU(xso + i * 512, bo) ^ tg;
        hx[4] = LDU(xso4, bo) ^ tg;
        if (ALLOK((bad(hx[0]) | bad(hx[1])) | (bad(hx[2]) | bad(hx[3])) | bad(hx[4]))) break;
      }
      STAMP(12);
      if (l & 16) hx[4] = v4u{0u, 0u, 0u, 0u};
      v4f s4 = asf4(hx[0]), q4 = s4 * s4;
#pragma unroll
      for (int i = 1; i < 5; ++i) { const v4f v = asf4(hx[i]); s4 += v; q4 += v * v; }
      float mb, sd; ln_finish((s4[0] + s4[1]) + (s4[2] + s4[3]), (q4[0] + q4[1]) + (q4[2] + q4[3]), mb, sd);
      float rsd = __builtin_amdgcn_rcpf(sd);
      v2f off = cs2 * mb;
      PINV(rsd); PINV(off);
      bar();
      STAMP(13);
      {
        const LAS v4f* gp4 = (const LAS v4f*)gpp;
        const v4f s = (gp4[0] + gp4[1]) + (gp4[2] + gp4[3]);
        const v2f s8 = *(const LAS v2f*)(gp4 + 4) - off;
        const u32 gu = pk((s[0] + s[2] + s8[0]) * rsd, (s[1] + s[3] + s8[1]) * rsd);
        const float gg = lo16(gu), uu = hi16(gu);
        const float hd = rbf(gg * __builtin_amdgcn_rcpf(1.f + ex2(-gg * LOG2E))) * uu;
        *(LAS unsigned short*)hwp = (unsigned short)pk(hd, hd);
      }
      bar();
      STAMP(14);
      FIN(); return;
    }
    if (isW) {
      const GAS char* op = (const GAS char*)wo + (size_t)((g * 2 + hh) * 128) * 1152 + cb3 * 384;
      LWAIT0(F_QK, 4);
#pragma unroll 1
      for (int gi = 0; gi < 12; ++gi) {
        WPUB(WINW - 4, N_V + gi * 4 - WINW + 4);
        const GAS char* s = op + ((gi & 3) * 36864 + (gi >> 2) * 128);
        const int d = L_T + (68 - 4 * gi) * 1024;
        DMA(s + o2, d, 0); DMA(s + ((o2 ^ 64) + 9216 - 1024), d, 1024); DMA(s + (o2 + 2 * 9216 - 2048), d, 2048); DMA(s + ((o2 ^ 64) + 3 * 9216 - 3072), d, 3072);
      }
      WPUB(0, N_W);
    } else {
      WPUB(24, N_V - 24); WPUB(16, N_V - 16); WPUB(8, N_V - 8); WPUB(0, N_V);
    }
    STAMP(11);
    if (!isR) { FIN(); return; }
    const u32 ep = *(LAS u32*)(L + L_EP);
    v4u tg = make_tag(ep);
    float xf = lo16(*(LAS unsigned short*)(L + L_X + r * 32 + (l & 3) * 8 + grp * 2));
    int go = (l >> 2) * 4608 + r * 64 + (l & 3) * 16;
    LAS char* frp = L + L_FR + (4 * (l & 3) + grp) * 48;
    GAS char* outp = (GAS char*)out + r * 32 + (l & 3) * 8 + grp * 2;
    PINV(tg); PINV(xf); PINV(go); PINV(frp); PINV(outp);
    if (isW) bar();
    float hv;
    {
      v4u p0, p1;
      GPOLL {
        CBAR();
        p0 = LDU(go, WS_RS) ^ tg; p1 = LDU(go, WS_RS + 16 * 4608) ^ tg;
        if (ALLOK(bad(p0) | bad(p1))) break;
      }
      STAMP(10);
      float s = tred4(asf4(p0) + asf4(p1));
      s += ror<4>(s); s += ror<8>(s);
      hv = rbf(xf + rbf(s));
      auto e = __builtin_amdgcn_permlane16_swap(asu(hv), asu(hv), false, false);
      auto e0 = __builtin_amdgcn_permlane32_swap(e[0], e[0], false, false);
      auto e1 = __builtin_amdgcn_permlane32_swap(e[1], e[1], false, false);
      STU((v4u{e0[0], e1[0], e0[1], e1[1]}) | tg, go, WS_BC);
      STAMP(9);
    }
    bar();
    bar();
    STAMP(14);
    {
      const v4f a0 = *(LAS v4f*)frp, a1 = *(LAS v4f*)(frp + 16); const float a2 = *(LAS float*)(frp + 32);
      const v4f a = a0 + a1;
      const float ov = hv + rbf(((a[0] + a[1]) + (a[2] + a[3])) + a2);
      if ((l & 12) == 0) *(GAS unsigned short*)outp = (unsigned short)pk(ov, ov);
      if (r == 0 && l == 0) __hip_atomic_store((GAS u32*)(ws + WS_EPOCH), ep + 1u, RLX);
    }
    STAMP(15);
    FIN(); return;
  }

  asm volatile("s_barrier" ::: "memory");
  __builtin_amdgcn_sched_barrier(0);
  const __amdgpu_buffer_rsrc_t rws = __builtin_amdgcn_make_buffer_rsrc(ws, 0, WS_END, 0x00020000);
  const int tq = (l >> 2) & 3, tp = (l & 3) * 8;
  u32 one = 1u, tmask = TMASK;
  PINV(fl); PINV(one); PINV(tmask);
  LWAIT(F_PROG, NX);
  v4u tg = make_tag(*(LAS u32*)(L + L_EP));
  PINV(tg);
  {
    const int rs = xcd >> 1, cbq = 2 * (t & 1) + (xcd & 1), gq_ = t >> 1;
    v4u xs[3];
    {
      LAS char* xp = L + L_X + (l >> 5) * 1152 + (l & 31) * 16;
      xs[0] = *(LAS v4u*)xp; xs[1] = *(LAS v4u*)(xp + 512);
      const v4u t2 = *(LAS v4u*)(xp + 1024 - ((l & 31) < 8 ? 0 : 1024));
      xs[2] = (l & 31) < 8 ? t2 : v4u{0u, 0u, 0u, 0u};
    }
    const v2u xa = *(LAS v2u*)(L + L_X + (l & 1) * 1152 + (rs * 144 + 16 * w + 4 * grp) * 2);
    float s = 0.f, ss = 0.f;
#pragma unroll
    for (int i = 0; i < 3; ++i)
#pragma unroll
      for (int k = 0; k < 4; ++k) { s = dot2(xs[i][k], 0x3F803F80u, s); ss = dot2(xs[i][k], xs[i][k], ss); }
    float mb, sd; ln_finish(s, ss, mb, sd);
    const float m0 = asf(__builtin_amdgcn_readlane(asu(mb), 0)), m1 = asf(__builtin_amdgcn_readlane(asu(mb), 32));
    const float d0 = asf(__builtin_amdgcn_readlane(asu(sd), 0)), d1 = asf(__builtin_amdgcn_readlane(asu(sd), 32));
    const float mm = (l & 1) ? m1 : m0, dd = __builtin_amdgcn_rcpf((l & 1) ? d1 : d0);
    v2u a;
#pragma unroll
    for (int k = 0; k < 2; ++k) { const u32 dlt = pk(lo16(xa[k]) - mm, hi16(xa[k]) - mm); a[k] = pk(lo16(dlt) * dd, hi16(dlt) * dd); }
    LAS char* qb = L + L_WQ + (2 * w + (grp >> 1)) * 1024 + (4 * (grp & 1) + tq) * 128 + tp;
    const int sw = ((tq >> 1) | (grp & 2)) << 5;
    LAS char* qb0 = qb + sw; LAS char* qb1 = qb + (32 ^ sw); LAS char* qb2 = qb + (64 ^ sw); LAS char* qb3 = qb + (96 ^ sw);
    LAS char* qw = L + L_QP + w * 512 + (l & 1) * 256 + (l & 6) * 32 + grp * 16;
    PINV(a); PINV(qb0); PINV(qb1); PINV(qb2); PINV(qb3); PINV(qw);
    LWAIT(F_PROG, 2 * w + 7);
    const v4f q0 = mfma16(trd(qb0), a, ZERO4), q1 = mfma16(trd(qb1), a, ZERO4), q2 = mfma16(trd(qb2), a, ZERO4), q3 = mfma16(trd(qb3), a, ZERO4);
    *(LAS v4f*)qw = sel4((l & 4) != 0, sel4((l & 2) != 0, q3, q2), sel4((l & 2) != 0, q1, q0));
    LINC(F_QP);
    STAMP(1);
    if (w == 8) {
      LAS char* qr = L + L_QP + (l & 31) * 16;
      int so = l < 32 ? grp * 4096 + (l & 15) * 16 : OOB, sb = WS_QG + gq_ * 8192 + rs * 1024 + cbq * 256;
      PINV(qr); PINV(so); PINS(sb);
      LWAIT0(F_QP, 9);
      const LAS v4f* q4p = (const LAS v4f*)qr;
      const v4f q4 = (((q4p[0] + q4p[32]) + (q4p[64] + q4p[96])) + ((q4p[128] + q4p[160]) + (q4p[192] + q4p[224]))) + q4p[256];
      const v4f q4r = asf4(__builtin_bit_cast(v4u, q4) + 0x200u);
      STU(ENC(q4r), so, sb);
      STAMP(2);
      int lo = l * 16, lb = WS_QG + g * 8192 + b * 4096;
      LAS char* qs = L + L_QS + l * 8;
      PINV(lo); PINS(lb); PINV(qs);
      LWAIT(F_PROG, QTRIG);
      v4u gq[4];
      GPOLL {
        CBAR();
#pragma unroll
        for (int i = 0; i < 4; ++i) gq[i] = LDU(lo + i * 1024, lb) ^ tg;
        if (ALLOK((bad(gq[0]) | bad(gq[1])) | (bad(gq[2]) | bad(gq[3])))) break;
      }
      const v4f qv = (asf4(gq[0]) + asf4(gq[1])) + (asf4(gq[2]) + asf4(gq[3]));
      *(LAS v2u*)qs = v2u{pk(qv[0], qv[1]), pk(qv[2], qv[3])};
      LINC(F_Q);
      STAMP(3);
      LWAIT(F_P, 4);
      const v4f ma = *(LAS v4f*)(L + L_WS + (l >> 5) * 32), la = *(LAS v4f*)(L + L_WS + (l >> 5) * 32 + 16);
      const float mc = fmaxf(fmaxf(ma[0], ma[1]), fmaxf(ma[2], ma[3]));
      const float lt = (la[0] * ex2((ma[0] - mc) * LOG2E) + la[1] * ex2((ma[1] - mc) * LOG2E)) + (la[2] * ex2((ma[2] - mc) * LOG2E) + la[3] * ex2((ma[3] - mc) * LOG2E));
      const v4f sq = {mc, lt, asf(__builtin_amdgcn_readlane(asu(mc), 32)), asf(__builtin_amdgcn_readlane(asu(lt), 32))};
      STU(ENC(sq), l == 0 ? 0 : OOB, WS_ST + g * 256 + u * 16);
      STAMP(5);
    }
  }
  if (w >= 4 && w < 8) {
    const int v = w - 4;
    const int kx = (l & 15) * 256 + ((grp ^ (l & 15)) << 4);
    const int ka = L_T + ((2 * v + rot8) & 7) * 8192 + kx, kb = L_T + ((2 * v + 1 + rot8) & 7) * 8192 + kx;
    LAS char* qa = L + L_QS + (l & 1) * 256 + grp * 16;
    LAS char* pw = L + L_P + (l >> 5) * 512 + v * 128 + (l & 31) * 2;
    LAS char* sp = L + L_WS + (l >> 5) * 32 + v * 4;
    PINV(qa); PINV(pw); PINV(sp);
    LWAIT(F_PROG, N_K);
    v4u kf[4][4];
#pragma unroll
    for (int ds = 0; ds < 4; ++ds) {
      kf[ds][0] = *(LAS v4u*)(L + (ka ^ (ds << 6))); kf[ds][1] = *(LAS v4u*)(L + (ka ^ (ds << 6)) + 4096);
      kf[ds][2] = *(LAS v4u*)(L + (kb ^ (ds << 6))); kf[ds][3] = *(LAS v4u*)(L + (kb ^ (ds << 6)) + 4096);
    }
#pragma unroll
    for (int ds = 0; ds < 4; ++ds) { PINV(kf[ds][0]); PINV(kf[ds][1]); PINV(kf[ds][2]); PINV(kf[ds][3]); }
    LINC(F_QK);
    LWAIT0(F_Q, 1);
    v4u qf[4];
#pragma unroll
    for (int ds = 0; ds < 4; ++ds) qf[ds] = *(LAS v4u*)(qa + ds * 64);
    v4f s[4] = {ZERO4, ZERO4, ZERO4, ZERO4};
#pragma unroll
    for (int ds = 0; ds < 4; ++ds)
#pragma unroll
      for (int kt = 0; kt < 4; ++kt) s[kt] = mfma(qf[ds], kf[ds][kt], s[kt]);
    const bool odd = (grp & 1) != 0, up = l >= 32;
    const float a0 = odd ? s[1][0] : s[0][0], a1 = odd ? s[1][1] : s[0][1], b0 = odd ? s[3][0] : s[2][0], b1 = odd ? s[3][1] : s[2][1];
    const u32 r1 = pk(up ? a1 : a0, up ? b1 : b0);
    const u32 r2 = pk(lo16(r1) * 0.08838834764831845f, hi16(r1) * 0.08838834764831845f);
    const float fa = lo16(r2), fb = hi16(r2);
    const float mx = max16x(rowmax(fmaxf(fa, fb)));
    const float pa = ex2((fa - mx) * LOG2E), pb = ex2((fb - mx) * LOG2E);
    const u32 pp = pk(pa, pb);
    *(LAS unsigned short*)pw = (unsigned short)pp;
    *(LAS unsigned short*)(pw + 64) = (unsigned short)(pp >> 16);
    float ls = rowsum(pa + pb); ls = t16(ls, ls);
    *(LAS float*)sp = mx; *(LAS float*)(sp + 16) = ls;
    LINC(F_P);
    STAMP(5);
  }
  if (w < 4) {
    const int vrow = (8 * grp + tq) * 256 + (l & 1) * 8;
    const int c0 = (4 * w + ((l >> 1) & 1)) ^ (tq << 2) ^ ((2 * grp) & 3);
    LAS char* va00 = L + L_V + vrow + (c0 << 4); LAS char* va01 = L + L_V + vrow + 1024 + ((c0 ^ 1) << 4);
    LAS char* va10 = L + L_V + vrow + ((c0 ^ 2) << 4); LAS char* va11 = L + L_V + vrow + 1024 + ((c0 ^ 3) << 4);
    LAS char* pa = L + L_P + (l & 1) * 512 + grp * 16;
    LAS char* mp = L + L_WS + (l & 1) * 32;
    int so = (l & 12) == 0 ? (l & 1) * 8192 + (l & 2) * 32 + grp * 16 : OOB, sb = WS_OG + g * 16384 + u * 512 + w * 128;
    PINV(va00); PINV(va01); PINV(va10); PINV(va11); PINV(pa); PINV(mp); PINV(so); PINS(sb);
    v4u va[8], vb[8];
#define VREAD(i) do { va[i] = cat(trd(va00 + (i) * 8192), trd(va01 + (i) * 8192)); vb[i] = cat(trd(va10 + (i) * 8192), trd(va11 + (i) * 8192)); } while (0)
    LWAIT(F_PROG, N_K + 32);
    VREAD(0); VREAD(1); VREAD(2); VREAD(3);
    LWAIT0(F_P, 4);
    STAMP(5);
    v4u pf[8];
#pragma unroll
    for (int i = 0; i < 8; ++i) pf[i] = *(LAS v4u*)(pa + i * 64);
    const v4f mg = *(LAS v4f*)mp;
    const float mc = fmaxf(fmaxf(mg[0], mg[1]), fmaxf(mg[2], mg[3]));
    float f0 = ex2((mg[0] - mc) * LOG2E), f1 = ex2((mg[1] - mc) * LOG2E), f2 = ex2((mg[2] - mc) * LOG2E), f3 = ex2((mg[3] - mc) * LOG2E);
    const bool n1 = (l & 2) != 0;
    v4f o = sel4(n1, mfma(vb[1], pf[1], mfma(vb[0], pf[0], ZERO4)), mfma(va[1], pf[1], mfma(va[0], pf[0], ZERO4))) * f0;
    o += sel4(n1, mfma(vb[3], pf[3], mfma(vb[2], pf[2], ZERO4)), mfma(va[3], pf[3], mfma(va[2], pf[2], ZERO4))) * f1;
    PINV(o); PINV(f2); PINV(f3);
    LWAIT0(F_PROG, N_K + 48);
    VREAD(4); VREAD(5);
    o += sel4(n1, mfma(vb[5], pf[5], mfma(vb[4], pf[4], ZERO4)), mfma(va[5], pf[5], mfma(va[4], pf[4], ZERO4))) * f2;
    PINV(o);
    LWAIT0(F_PROG, N_K + 56);
    VREAD(6);
    v4f ta = mfma(va[6], pf[6], ZERO4), tb = mfma(vb[6], pf[6], ZERO4);
    LWAIT0(F_PROG, N_V);
    VREAD(7);
    o += sel4(n1, mfma(vb[7], pf[7], tb), mfma(va[7], pf[7], ta)) * f3;
    STU(ENC(o), so, sb);
    LINC(F_PV);
    STAMP(7);
  }
  const int t8 = tq * 128 + tp, sw8 = ((tq >> 1) | ((grp & 1) << 1)) << 5;
  if (isW) {
    const int hq = g * 2 + hh;
    if (w >= 4 && w < 8) {
      LWAIT(F_PROG, STRIG);
      v4u s0, s1;
      GPOLL {
        CBAR();
        s0 = LDU((l >> 3) * 16, WS_ST + g * 256) ^ tg; s1 = LDU((l >> 3) * 16 + 128, WS_ST + g * 256) ^ tg;
        if (ALLOK(bad(s0) | bad(s1))) break;
      }
      float wn[2];
#pragma unroll
      for (int i = 0; i < 2; ++i) {
        const v4u st = i ? s1 : s0;
        const float mj = asf(hh ? st[2] : st[0]), lj = asf(hh ? st[3] : st[1]);
        float M = fmaxf(mj, ror<8>(mj)); M = max32x(max16x(M));
        const float e = ex2((mj - M) * LOG2E);
        float Ls = e * lj; Ls += ror<8>(Ls); Ls = t16(Ls, Ls); Ls = t32(Ls, Ls);
        wn[i] = e * __builtin_amdgcn_rcpf(Ls);
      }
      int lo = (l >> 3) * 512 + (l & 7) * 16, lb = WS_OG + hq * 8192 + (w - 4) * 128;
      LAS char* ob = L + L_OB + ((l >> 3) & 1) * 256 + (w - 4) * 64 + (l & 7) * 8 + grp * 2;
      PINV(wn[0]); PINV(wn[1]); PINV(lo); PINS(lb); PINV(ob);
      LWAIT0(F_PV, 4);
      v4u o0, o1;
      GPOLL {
        CBAR();
        o0 = LDU(lo, lb) ^ tg; o1 = LDU(lo, lb + 4096) ^ tg;
        if (ALLOK(bad(o0) | bad(o1))) break;
      }
      STAMP(8);
      const float r0 = tred4(asf4(o0) * wn[0]), r1 = tred4(asf4(o1) * wn[1]);
      const bool hi8 = (l & 8) != 0;
      const float val = (hi8 ? r1 : r0) + ror<8>(hi8 ? r0 : r1);
      *(LAS unsigned short*)ob = (unsigned short)pk(val, val);
      bar();
    } else if (w < 4) {
      LAS char* of = L + L_OB + (l & 1) * 256 + grp * 16;
      int so = (l & 15) < 6 ? (l & 1) * 2304 + (l & 6) * 32 + grp * 16 : OOB, sb = WS_RS + hq * 4608 + cb3 * 768 + w * 192;
      PINV(of); PINV(so); PINS(sb);
      LWAIT(F_PROG, N_W);
      v4u wf[3][4];
#pragma unroll
      for (int n = 0; n < 3; ++n) {
        LAS char* wp = L + L_T + (56 - 16 * ((3 * w + n) >> 2) + grp) * 1024 + t8 + ((((3 * w + n) & 3) << 5) ^ sw8);
#pragma unroll
        for (int ks = 0; ks < 4; ++ks) { wf[n][ks] = trd2(wp + (3 - ks) * 4096, 512); PINV(wf[n][ks]); }
      }
      bar();
      v4f y[3] = {ZERO4, ZERO4, ZERO4};
      v4u oq[4];
#pragma unroll
      for (int ks = 0; ks < 4; ++ks) oq[ks] = *(LAS v4u*)(of + ks * 64);
#pragma unroll
      for (int ks = 0; ks < 4; ++ks)
#pragma unroll
        for (int n = 0; n < 3; ++n) y[n] = mfma(wf[n][ks], oq[ks], y[n]);
      const v4f yv = sel4((l & 4) != 0, y[2], sel4((l & 2) != 0, y[1], y[0]));
      STU(ENC(yv), so, sb);
      STAMP(9);
    } else {
      bar();
    }
  }
  if (!isM) {
    if (isR) {
      int go = (l >> 2) * 4608 + r * 64 + (l & 3) * 16, gb = WS_H4 + w * 16 * 4608;
      LAS char* fr = L + L_FR + (4 * (l & 3) + grp) * 48 + w * 4;
      PINV(go); PINS(gb); PINV(fr);
      bar();
      v4u p;
      GPOLL {
        CBAR();
        p = LDU(go, gb) ^ tg;
        if (ALLOK(bad(p))) break;
      }
      STAMP(12);
      float s = tred4(asf4(p));
      s += ror<4>(s); s += ror<8>(s);
      *(LAS float*)fr = s;
      bar();
    }
    FIN(); return;
  }
  {
    LWAIT(F_PROG, N_T3);
    STAMP(11);
    v2u gf[4][2][2];
#pragma unroll
    for (int kb = 0; kb < 4; ++kb) {
      const int rg = 4 * w + kb;
      const int base = (rg < 18 ? L_T + (71 - 2 * rg) * 1024 : L_V + (2 * rg - 36) * 1024) + (4 * grp + tq) * 64 + tp, ms = rg < 18 ? -1024 : 1024;
#pragma unroll
      for (int mat = 0; mat < 2; ++mat)
#pragma unroll
        for (int nt = 0; nt < 2; ++nt) gf[kb][mat][nt] = trd(L + base + mat * ms + ((nt ^ (grp >> 1)) << 5));
    }
    const bool n1 = l >= 32;
    const int grow = (grp & 1) * 32 + (grp >> 1) * 16 + (l & 15);
    {
      const v2u ones = {0x3F803F80u, 0x3F803F80u};
      v4f cs[2][2];
#pragma unroll
      for (int mat = 0; mat < 2; ++mat)
#pragma unroll
        for (int nt = 0; nt < 2; ++nt) cs[mat][nt] = mfma16(ones, gf[3][mat][nt], mfma16(ones, gf[2][mat][nt], mfma16(ones, gf[1][mat][nt], mfma16(ones, gf[0][mat][nt], ZERO4))));
      if ((grp & 1) == 0) *(LAS v2f*)(L + L_SP + grow * 80 + w * 8) = v2f{n1 ? cs[0][1][0] : cs[0][0][0], n1 ? cs[1][1][0] : cs[1][0][0]};
    }
    int ho = ((l >> 2) & 1) * 2304 + (64 * w + 16 * (l & 3) + 4 * grp) * 4, bo = WS_BC + (xcd * 2 + (t & 1)) * BC_STRIDE;
    LAS char* gw = L + L_GP + grow * 80 + w * 8;
    LAS char* hr = L + L_HW + (l & 1) * 64 + grp * 16;
    int so = (l & 8) == 0 ? (l & 1) * 2304 + (l & 6) * 32 + grp * 16 : OOB, sb = WS_H4 + m * 4608 + w * 256;
    PINV(ho); PINS(bo); PINV(gw); PINV(hr); PINV(so); PINS(sb);
    bar();
    v4u df[4];
#pragma unroll
    for (int nt = 0; nt < 4; ++nt) { df[nt] = trd2(L + L_T + (4 * w + grp) * 1024 + t8 + ((nt << 5) ^ sw8), 512); }
    v4u hu;
    GPOLL {
      CBAR();
      hu = LDU(ho, bo);
      if (ALLOK(bad(hu ^ tg))) break;
    }
    STAMP(12);
    const v2u ha = {__builtin_amdgcn_perm(hu[1], hu[0], 0x07060302u), __builtin_amdgcn_perm(hu[3], hu[2], 0x07060302u)};
    float y[2][2];
#pragma unroll
    for (int mat = 0; mat < 2; ++mat)
#pragma unroll
      for (int nt = 0; nt < 2; ++nt) {
        const v4f a0 = mfma16(ha, gf[0][mat][nt], ZERO4), a1 = mfma16(ha, gf[1][mat][nt], ZERO4), a2 = mfma16(ha, gf[2][mat][nt], ZERO4), a3 = mfma16(ha, gf[3][mat][nt], ZERO4);
        y[mat][nt] = (a0[0] + a1[1]) + (a2[2] + a3[3]);
      }
    *(LAS v2f*)gw = v2f{n1 ? y[0][1] : y[0][0], n1 ? y[1][1] : y[1][0]};
    bar();
    STAMP(13);
    bar();
    const v4u da = *(LAS v4u*)hr;
    v4f d[4];
#pragma unroll
    for (int nt = 0; nt < 4; ++nt) d[nt] = mfma(df[nt], da, ZERO4);
    const v4f dv = sel4((l & 4) != 0, sel4((l & 2) != 0, d[3], d[2]), sel4((l & 2) != 0, d[1], d[0]));
    STU(ENC(dv), so, sb);
    STAMP(14);
  }
  FIN();
}

__global__ void __launch_bounds__(256) dbg_spin(const u64* p) {
  __shared__ u64 smn[256], smx[256], sv[256], sall[256]; __shared__ int sn[256];
  const int c = threadIdx.x;
  u64 mn = ~0ull, all = 0;
  for (int w = 0; w < 10; ++w) { u64 v = p[(c * 16 + w) * 16]; if (v && v < mn) mn = v; for (int s = 0; s < 16; ++s) { u64 q = p[(c * 16 + w) * 16 + s]; if (q > all) all = q; } }
  smn[c] = mn; sv[c] = p[(c * 16 + 9) * 16 + 0]; sall[c] = all;
  __syncthreads();
  if (c == 0) {
    u64 t0 = ~0ull, t1 = 0; for (int i = 0; i < 256; ++i) { if (smn[i] < t0) t0 = smn[i]; if (sall[i] > t1) t1 = sall[i]; }
    u64 vmx = 0, vmn = ~0ull, sum = 0; int n = 0;
    for (int i = 0; i < 256; ++i) if (sv[i]) { u64 d = sv[i] - t0; if (d > vmx) vmx = d; if (d < vmn) vmn = d; sum += d; ++n; }
    const int K = 4;
    u64 V = K == 0 ? vmx : K == 1 ? vmn : K == 2 ? sum / (n ? n : 1) : K == 3 ? t1 - t0 : 0;
    if (V > 100000) V = 100000;
    const u64 until = t1 + 20000 + V * 20;
    while (__builtin_amdgcn_s_memrealtime() < until) __builtin_amdgcn_s_sleep(4);
  }
}
extern "C" void kernel_launch(void* const* d_in, const int* in_sizes, int n_in, void* d_out, int out_size, void* d_ws, size_t ws_size, hipStream_t stream) {
  (void)hipFuncSetAttribute((const void*)fused_decode, hipFuncAttributeMaxDynamicSharedMemorySize, L_END);
  fused_decode<<<256, 640, L_END, stream>>>((const char*)d_in[0], (char*)d_ws, (const char*)d_in[3], (const char*)d_in[1], (const char*)d_in[2], (const char*)d_in[4],
                                           (const char*)d_in[6], (const char*)d_in[7], (const char*)d_in[8], (char*)d_out);
  dbg_spin<<<1, 256, 0, stream>>>((const u64*)((char*)d_ws + STAMP_OFF));
}
```

```cpp
#include <hip/hip_runtime.h>
#include <stdint.h>

#define GAS __attribute__((address_space(1)))
#define LAS __attribute__((address_space(3)))
typedef unsigned u32;
typedef unsigned long long u64;
typedef unsigned v4u __attribute__((ext_vector_type(4)));
typedef unsigned v2u __attribute__((ext_vector_type(2)));
typedef float v4f __attribute__((ext_vector_type(4)));
typedef float v2f __attribute__((ext_vector_type(2)));
typedef __bf16 v8bf __attribute__((ext_vector_type(8)));
typedef __bf16 v2bf __attribute__((ext_vector_type(2)));
typedef short v4s __attribute__((ext_vector_type(4)));

#define WIN 24
#define WINW 24
#define QTRIG 56
#define STRIG 140
#define GSHIFT 2
#define PSLEEP 4
#define SWIN 14
#define HELP 1
#define PFMODE 0
#define CODE_KB 15

constexpr int WS_EPOCH = 0, WS_ST = 4096, WS_QG = WS_ST + 32768, WS_OG = WS_QG + 131072, WS_RS = WS_OG + 262144, WS_BC = WS_RS + 32 * 4608, BC_STRIDE = 4608,
              WS_H4 = WS_BC + 16 * BC_STRIDE, WS_END = WS_H4 + 144 * 4608;
constexpr int L_T = 0, L_WQ = 65536, L_GP = 73728, L_SP = L_GP + 5120, L_X = 83968, L_JUNK = 87040, L_V = 88064, L_FLAG = L_V + 65536, L_EP = L_FLAG + 64,
              L_QP = L_EP + 256, L_QS = L_QP + 4608, L_P = L_QS + 512, L_WS = L_P + 1024, L_OB = L_WS + 64, L_HW = L_OB + 512, L_FR = L_HW + 128, L_WN = L_FR + 768, L_END = L_WN + 64;
static_assert(L_END <= 163840 && L_SP + 2560 <= L_X && L_WQ + 18432 <= L_X, "LDS");
constexpr int F_PROG = 0, F_QP = 1, F_Q = 2, F_QK = 3, F_P = 4, F_PV = 5, F_WN = 6, F_LN = 7, F_L = 8, F_CS = 9;
constexpr int NX = 4, N0 = 24, N_K = N0 + 64, N_V = N_K + 64, N_W = N_V + 48, N_T3 = 400;
constexpr int SC1 = 16, NT = 2;
constexpr u32 GSPIN = 1u << 17, LSPIN = 1u << 22;
constexpr int OOB = 0x7ffffff0;
constexpr u32 TMASK = 0x3FFu;

#define STAMP(i)
#define FIN()

#define CBAR() asm volatile("" ::: "memory")
#define PINV(x) asm volatile("" : "+v"(x))
#define PINS(x) asm volatile("" : "+s"(x))
#define GPOLL _Pragma("nounroll") for (u32 sp_ = 0; sp_ < GSPIN; ++sp_)
#define RLX __ATOMIC_RELAXED, __HIP_MEMORY_SCOPE_AGENT
__device__ __forceinline__ void bar() { asm volatile("s_waitcnt lgkmcnt(0)\n\ts_barrier" ::: "memory"); __builtin_amdgcn_sched_barrier(0); }
__device__ __forceinline__ u32 pk(float lo, float hi) { return __builtin_bit_cast(u32, __builtin_convertvector(v2f{lo, hi}, v2bf)); }
__device__ __forceinline__ float asf(u32 u) { return __builtin_bit_cast(float, u); }
__device__ __forceinline__ u32 asu(float f) { return __builtin_bit_cast(u32, f); }
__device__ __forceinline__ v4f asf4(v4u u) { return __builtin_bit_cast(v4f, u); }
__device__ __forceinline__ float lo16(u32 u) { return asf(u << 16); }
__device__ __forceinline__ float hi16(u32 u) { return asf(u & 0xffff0000u); }
__device__ __forceinline__ float rbf(float f) { return lo16(pk(f, f)); }
__device__ __forceinline__ v4f mfma(v4u a, v4u b, v4f c) { return __builtin_amdgcn_mfma_f32_16x16x32_bf16(__builtin_bit_cast(v8bf, a), __builtin_bit_cast(v8bf, b), c, 0, 0, 0); }
__device__ __forceinline__ v4f mfma16(v2u a, v2u b, v4f c) { return __builtin_amdgcn_mfma_f32_16x16x16bf16_1k(__builtin_bit_cast(v4s, a), __builtin_bit_cast(v4s, b), c, 0, 0, 0); }
__device__ __forceinline__ float dot2(u32 a, u32 b, float c) { return __builtin_amdgcn_fdot2_f32_bf16(__builtin_bit_cast(v2bf, a), __builtin_bit_cast(v2bf, b), c, false); }
template <int C> __device__ __forceinline__ float dpp(float x) { return asf(__builtin_amdgcn_mov_dpp(asu(x), C, 0xf, 0xf, true)); }
template <int N> __device__ __forceinline__ float ror(float x) { return asf(__builtin_amdgcn_mov_dpp(asu(x), 0x120 + N, 0xf, 0xf, true)); }
__device__ __forceinline__ float t16(float x, float y) { auto s = __builtin_amdgcn_permlane16_swap(asu(x), asu(y), false, false); return asf(s[0]) + asf(s[1]); }
__device__ __forceinline__ float t32(float x, float y) { auto s = __builtin_amdgcn_permlane32_swap(asu(x), asu(y), false, false); return asf(s[0]) + asf(s[1]); }
__device__ __forceinline__ float tred4(v4f v) { return t16(t32(v[0], v[2]), t32(v[1], v[3])); }
__device__ __forceinline__ float max16x(float x) { auto s = __builtin_amdgcn_permlane16_swap(asu(x), asu(x), false, false); return fmaxf(asf(s[0]), asf(s[1])); }
__device__ __forceinline__ float max32x(float x) { auto s = __builtin_amdgcn_permlane32_swap(asu(x), asu(x), false, false); return fmaxf(asf(s[0]), asf(s[1])); }
__device__ __forceinline__ float rowsum(float x) { x += ror<8>(x); x += ror<4>(x); x += ror<2>(x); x += ror<1>(x); return x; }
__device__ __forceinline__ float rowmax(float x) { x = fmaxf(x, ror<8>(x)); x = fmaxf(x, ror<4>(x)); x = fmaxf(x, ror<2>(x)); x = fmaxf(x, ror<1>(x)); return x; }
__device__ __forceinline__ float ex2(float x) { return __builtin_amdgcn_exp2f(x); }
__device__ __forceinline__ v2u trd(LAS char* p) { return __builtin_bit_cast(v2u, __builtin_amdgcn_ds_read_tr16_b64_v4i16((LAS v4s*)p)); }
__device__ __forceinline__ v4u cat(v2u a, v2u b) { return v4u{a[0], a[1], b[0], b[1]}; }
__device__ __forceinline__ v4u trd2(LAS char* p, int d) { return cat(trd(p), trd(p + d)); }
__device__ __forceinline__ v4f sel4(bool c, v4f a, v4f b) { return v4f{c ? a[0] : b[0], c ? a[1] : b[1], c ? a[2] : b[2], c ? a[3] : b[3]}; }
__device__ __forceinline__ u32 bfi(u32 mask, u32 a, u32 b) { return (mask & a) | (~mask & b); }
#define ENC(v) (v4u{bfi(tmask, tg[0], asu((v)[0])), bfi(tmask, tg[1], asu((v)[1])), bfi(tmask, tg[2], asu((v)[2])), bfi(tmask, tg[3], asu((v)[3]))})
__device__ __forceinline__ u32 bad(v4u y) { return (y[0] | y[1]) | (y[2] | y[3]); }
#define ALLOK(x) (__builtin_amdgcn_ballot_w64(((x) & TMASK) != 0u) == 0ull)
constexpr float LOG2E = 1.4426950408889634f;
#define ZERO4 (v4f{0.f, 0.f, 0.f, 0.f})

__device__ __forceinline__ void ln_finish(float s, float ss, float& mean_bf, float& sd_bf) {
  float z = rowsum(t16(s, ss));
  auto e = __builtin_amdgcn_permlane16_swap(asu(z), asu(z), false, false);
  const float m = asf(e[0]) * (1.f / 576.f);
  const float var = fmaxf(asf(e[1]) * (1.f / 576.f) - m * m, 0.f);
  mean_bf = rbf(m);
  sd_bf = rbf(__builtin_amdgcn_sqrtf(rbf(rbf(var) + 1e-5f)));
}
#define WPUB(A, val) asm volatile("s_waitcnt vmcnt(%0)\n\tds_write_b32 %1, %2" :: "i"(A), "v"(fl), "v"(val) : "memory")
template <int F, int SLEEP> __device__ __forceinline__ void lwait(u32 fl, u32 n) {
#pragma nounroll
  for (u32 sp = 0; sp < LSPIN; ++sp) {
    u32 v; asm volatile("ds_read_b32 %0, %1 offset:%2\n\ts_waitcnt lgkmcnt(0)" : "=v"(v) : "v"(fl), "i"(4 * F) : "memory");
    if ((u32)__builtin_amdgcn_readfirstlane(v) >= n) break;
    if (SLEEP) __builtin_amdgcn_s_sleep(SLEEP);
  }
  __builtin_amdgcn_sched_barrier(0);
}
#define LWAIT(F, n) lwait<F, 1>(fl, (u32)(n))
#define LWAIT0(F, n) lwait<F, 0>(fl, (u32)(n))
#define LINC(F) asm volatile("s_mov_b64 exec, 1\n\tds_add_u32 %0, %1 offset:%2\n\ts_mov_b64 exec, -1" :: "v"(fl), "v"(one), "i"(4 * (F)) : "memory")
#define DMA0(src, dst, imm) __builtin_amdgcn_global_load_lds((const GAS u32*)(src), (LAS u32*)(L + (dst)), 16, imm, 0)
#define DMA(src, dst, imm) __builtin_amdgcn_global_load_lds((const GAS u32*)(src), (LAS u32*)(L + (dst)), 16, imm, NT)
#define LDU(off, soff) __builtin_amdgcn_raw_buffer_load_b128(rws, (off), (soff), SC1)
#define STU(v, off, soff) __builtin_amdgcn_raw_buffer_store_b128((v), rws, (off), (soff), SC1)
__device__ __forceinline__ v4u make_tag(u32 ep) { const u32 t = (ep + 1u) * 0x2F5u; return v4u{t & TMASK, (t + 341u) & TMASK, (t + 682u) & TMASK, (t + 1023u) & TMASK}; }

__global__ void __launch_bounds__(640) fused_decode(const char* __restrict__ x, char* __restrict__ ws, const char* __restrict__ wq, const char* __restrict__ kc,
                                                    const char* __restrict__ vc, const char* __restrict__ wo, const char* __restrict__ w1,
                                                    const char* __restrict__ w3, const char* __restrict__ w2, char* __restrict__ out) {
  extern __shared__ __attribute__((aligned(16))) char lds_[];
  LAS char* L = (LAS char*)lds_;
  u64 pc0; asm volatile("s_getpc_b64 %0" : "=s"(pc0));
  const int l = threadIdx.x & 63, wid = __builtin_amdgcn_readfirstlane(threadIdx.x >> 6), w = wid - 1;
  const int c = blockIdx.x, xcd = c & 7, t = c >> 3, g = xcd * 2 + (t >> 4), u = t & 15, b = u >> 3, j = u & 7;
  const int grp = l >> 4;
  const bool isM = u < 9, isW = u >= 9 && u < 15;
  const int m = g * 9 + u, r = g * 6 + (u - 9), hh = u >= 12 ? 1 : 0, cb3 = u - 9 - 3 * hh;
  const bool isR = isW && r < 72;
  const int gw_ = (g + GSHIFT) & 15, hq = gw_ * 2 + hh;
  const int rot = (c + (c >> 4)) & 15, rot8 = rot >> 1;
  u32 fl = L_FLAG;
  STAMP(0);
  if (threadIdx.x < 16) asm volatile("ds_write_b32 %0, %1" :: "v"(L_FLAG + l * 4), "v"(0) : "memory");
  asm volatile("" :: "s"(x), "s"(ws), "s"(wq), "s"(kc), "s"(vc), "s"(wo), "s"(w1), "s"(w3), "s"(w2), "s"(out));
  bar();

  if (__builtin_expect(wid == 0, 1)) {
    __builtin_amdgcn_s_setprio(3);
    const u32 p8r = l >> 3, p8c = ((l & 7) ^ ((l >> 3) & 2)) << 4;
    {
      DMA0((const GAS char*)x + l * 16, L_X, 0); DMA0((const GAS char*)x + l * 16, L_X, 1024); DMA0((const GAS char*)x + (l & 15) * 16, L_X, 2048);
      __builtin_amdgcn_global_load_lds((const GAS u32*)(ws + WS_EPOCH), (LAS u32*)(L + L_EP), 4, 0, SC1);
      DMA0((pc0 & ~255ull) + (u < CODE_KB ? u : CODE_KB - 1) * 1024 + l * 16, L_JUNK, 0);
      const GAS char* qp = (const GAS char*)wq + (size_t)((xcd >> 1) * 144) * 8192 + t * 256 + (xcd & 1) * 128;
      const u32 o = p8r * 8192 + p8c, o1 = (o ^ 64) + 65536 - 1024;
#pragma unroll 1
      for (int i = 0; i < 9; ++i) { const GAS char* s_ = qp + i * 131072; DMA(s_ + o, L_WQ + i * 2048, 0); DMA(s_ + o1, L_WQ + i * 2048, 1024); }
      {
        const char* tb = (l & 8) ? ((l & 4) ? wo : w2) : ((l & 4) ? w3 : w1);
        tb += ((l & 3) == 3 ? 0 : (l & 3)) * 2097152;
        if (l >= 16) tb = vc + ((size_t)(b * 16 + g) * 8192 + 6144 + j * 256) * 256;
        __builtin_amdgcn_global_load_lds((const GAS u32*)tb, (LAS u32*)(L + L_JUNK), 4, 0, 0);
      }
    }
    const size_t reg = ((size_t)(b * 16 + g) * 8192 + 6144 + j * 256) * 256;
    const GAS char* kp = (const GAS char*)kc + reg;
    const u32 kv0 = grp * 256 + (((l & 15) ^ grp) << 4);
#define KGRP(gi) do { const u32 go_ = (((gi) + rot) & 15) * 4096; const GAS char* s_ = kp + go_; \
      DMA(s_ + kv0, L_T + go_, 0); DMA(s_ + (kv0 ^ 64), L_T + go_, 1024); DMA(s_ + (kv0 ^ 128), L_T + go_, 2048); DMA(s_ + (kv0 ^ 192), L_T + go_, 3072); } while (0)
    KGRP(0);
    WPUB(N0 + 4 - NX, NX);
    STAMP(3);
#define PACE(n) do { if (WINW != WIN && isW) WPUB(WINW - 4, (n) - WINW + 4 > NX ? (n) - WINW + 4 : NX); else WPUB(WIN - 4, (n) - WIN + 4 > NX ? (n) - WIN + 4 : NX); } while (0)
#pragma unroll 1
    for (int gi = 1; gi < 16; ++gi) { PACE(N0 + gi * 4); KGRP(gi); }
    STAMP(4);
    const GAS char* vp = (const GAS char*)vc + reg;
    const u32 vv0 = grp * 256 + (((l & 15) ^ (grp << 2)) << 4);
#pragma unroll 1
    for (int gi = 0; gi < 16; ++gi) {
      PACE(N_K + gi * 4);
      const GAS char* s = vp + (((gi + 2 * rot8) & 15) ^ (rot & 1)) * 4096; const int d = L_V + (gi ^ (rot & 1)) * 4096;
      DMA(s + vv0, d, 0); DMA(s + (vv0 ^ 16), d, 1024); DMA(s + (vv0 ^ 32), d, 2048); DMA(s + (vv0 ^ 48), d, 3072);
    }
    STAMP(5);
    const __amdgpu_buffer_rsrc_t rws = __builtin_amdgcn_make_buffer_rsrc(ws, 0, WS_END, 0x00020000);
    const u32 o2 = p8r * 1152 + p8c;
    LWAIT0(F_QP, 9);
    if (isM) {
      const u32 o13 = (l >> 2) * 9216 + (((l & 3) ^ ((l >> 4) & 2)) << 4) + m * 64;
#define STRIP(rg, dst) do { DMA((const GAS char*)w1 + (size_t)(rg) * 147456 + o13, (dst), 0); DMA((const GAS char*)w3 + (size_t)(rg) * 147456 + o13, (dst) + ((rg) < 18 ? -1024 : 1024), 0); } while (0)
      LWAIT0(F_QK, 4);
      WPUB(24, N_V - 24); WPUB(16, N_V - 16); STRIP(0, L_T + 71 * 1024); STRIP(1, L_T + 69 * 1024);
      WPUB(12, N_V - 8); STRIP(2, L_T + 67 * 1024); STRIP(3, L_T + 65 * 1024);
      WPUB(8, N_V);
      STAMP(6);
#pragma unroll 1
      for (int rg = 4; rg < 18; ++rg) { asm volatile("s_waitcnt vmcnt(%0)" :: "i"(SWIN) : "memory"); STRIP(rg, L_T + (71 - 2 * rg) * 1024); }
      LWAIT0(F_PV, 4);
#pragma unroll 1
      for (int rg = 18; rg < 36; ++rg) { asm volatile("s_waitcnt vmcnt(%0)" :: "i"(SWIN) : "memory"); STRIP(rg, L_V + (2 * rg - 36) * 1024); }
      const GAS char* dp = (const GAS char*)w2 + (size_t)m * 36864;
#pragma unroll 1
      for (int i = 0; i < 9; ++i) {
        asm volatile("s_waitcnt vmcnt(56)" ::: "memory");
        const GAS char* s = dp + i * 128;
        DMA(s + o2, L_T + i * 4096, 0); DMA(s + ((o2 ^ 64) + 9216 - 1024), L_T + i * 4096, 1024);
        DMA(s + (o2 + 2 * 9216 - 2048), L_T + i * 4096, 2048); DMA(s + ((o2 ^ 64) + 3 * 9216 - 3072), L_T + i * 4096, 3072);
      }
      WPUB(36, N_T3);
      STAMP(10);
      int xso = (l >> 5) * 2304 + (l & 31) * 16, xso4 = (l >> 5) * 2304 + (l & 15) * 16 + 2048, bo = WS_BC + (xcd * 2 + (t & 1)) * BC_STRIDE;
      LAS char* gpp = L + L_GP + l * 80; LAS char* hwp = L + L_HW + l * 2;
      LWAIT0(F_CS, 9);
      v4u tg; v2f cs2;
      {
        u32 ev; v4f s0, s1, s2, s3; v2f s8;
        asm volatile("ds_read_b32 %0, %6\n\tds_read_b128 %1, %7\n\tds_read_b128 %2, %7 offset:16\n\tds_read_b128 %3, %7 offset:32\n\tds_read_b128 %4, %7 offset:48\n\t"
                     "ds_read_b64 %5, %7 offset:64\n\ts_waitcnt lgkmcnt(0)"
                     : "=&v"(ev), "=&v"(s0), "=&v"(s1), "=&v"(s2), "=&v"(s3), "=&v"(s8) : "v"(L_EP), "v"(L_SP + (l & 31) * 80) : "memory");
        tg = make_tag(ev);
        const v4f s = (s0 + s1) + (s2 + s3);
        cs2 = v2f{s[0] + s[2] + s8[0], s[1] + s[3] + s8[1]};
      }
      PINV(cs2); PINV(tg); PINV(xso); PINV(xso4); PINS(bo); PINV(gpp); PINV(hwp);
      v4u hx[5];
      GPOLL {
        CBAR();
#pragma unroll
        for (int i = 0; i < 4; ++i) hx[i] = LDU(xso + i * 512, bo) ^ tg;
        hx[4] = LDU(xso4, bo) ^ tg;
        if (ALLOK((bad(hx[0]) | bad(hx[1])) | (bad(hx[2]) | bad(hx[3])) | bad(hx[4]))) break;
      }
      STAMP(12);
      if (l & 16) hx[4] = v4u{0u, 0u, 0u, 0u};
      v4f s4 = asf4(hx[0]), q4 = s4 * s4;
#pragma unroll
      for (int i = 1; i < 5; ++i) { const v4f v = asf4(hx[i]); s4 += v; q4 += v * v; }
      float mb, sd; ln_finish((s4[0] + s4[1]) + (s4[2] + s4[3]), (q4[0] + q4[1]) + (q4[2] + q4[3]), mb, sd);
      float rsd = __builtin_amdgcn_rcpf(sd);
      v2f off = cs2 * mb;
      PINV(rsd); PINV(off);
      bar();
      STAMP(13);
      {
        const LAS v4f* gp4 = (const LAS v4f*)gpp;
        const v4f s = (gp4[0] + gp4[1]) + (gp4[2] + gp4[3]);
        const v2f s8 = *(const LAS v2f*)(gp4 + 4) - off;
        const u32 gu = pk((s[0] + s[2] + s8[0]) * rsd, (s[1] + s[3] + s8[1]) * rsd);
        const float gg = lo16(gu), uu = hi16(gu);
        const float hd = rbf(gg * __builtin_amdgcn_rcpf(1.f + ex2(-gg * LOG2E))) * uu;
        *(LAS unsigned short*)hwp = (unsigned short)pk(hd, hd);
      }
      bar();
      STAMP(14);
      FIN(); return;
    }
    if (isW) {
      const GAS char* op = (const GAS char*)wo + (size_t)(hq * 128) * 1152 + cb3 * 384;
      LWAIT0(F_QK, 4);
#pragma unroll 1
      for (int gi = 0; gi < 12; ++gi) {
        WPUB(WINW - 4, N_V + gi * 4 - WINW + 4);
        const GAS char* s = op + ((gi & 3) * 36864 + (gi >> 2) * 128);
        const int d = L_T + (68 - 4 * gi) * 1024;
        DMA(s + o2, d, 0); DMA(s + ((o2 ^ 64) + 9216 - 1024), d, 1024); DMA(s + (o2 + 2 * 9216 - 2048), d, 2048); DMA(s + ((o2 ^ 64) + 3 * 9216 - 3072), d, 3072);
      }
      WPUB(0, N_W);
    } else {
      const GAS char* wm = (const GAS char*)((t & 16) ? w3 : w1);
      const u32 c3 = (3u - (u32)((u64)wm >> 7) - 9u * xcd) & 7u;
      const GAS char* pp = wm + (u32)((575 - l) * 9216 + (9 * xcd + c3) * 128);
#define PFI(p, imm) __builtin_amdgcn_global_load_lds((const GAS u32*)(p), (LAS u32*)(L + L_JUNK - (imm)), 4, (imm), 0)
      WPUB(24, N_V - 24); WPUB(16, N_V - 16); PFI(pp, 0);
      WPUB(9, N_V - 8); PFI(pp - 589824, 0);
      WPUB(2, N_V);
      if (c3 == 0u) { PFI(pp, 1024); PFI(pp - 589824, 1024); }
      LWAIT0(F_PV, 4);
#pragma unroll 1
      for (int i = 2; i < 9; ++i) { PFI(pp - i * 589824, 0); if (c3 == 0u) PFI(pp - i * 589824, 1024); }
      asm volatile("s_waitcnt vmcnt(0)" ::: "memory");
    }
    STAMP(11);
    if (!isR) { FIN(); return; }
    const u32 ep = *(LAS u32*)(L + L_EP);
    v4u tg = make_tag(ep);
    float xf = lo16(*(LAS unsigned short*)(L + L_X + r * 32 + (l & 3) * 8 + grp * 2));
    int go = (l >> 2) * 4608 + r * 64 + (l & 3) * 16;
    LAS char* frp = L + L_FR + (4 * (l & 3) + grp) * 48;
    GAS char* outp = (GAS char*)out + r * 32 + (l & 3) * 8 + grp * 2;
    PINV(tg); PINV(xf); PINV(go); PINV(frp); PINV(outp);
    bar();
    float hv;
    {
      v4u p0, p1;
      GPOLL {
        CBAR();
        p0 = LDU(go, WS_RS) ^ tg; p1 = LDU(go, WS_RS + 16 * 4608) ^ tg;
        if (ALLOK(bad(p0) | bad(p1))) break;
      }
      STAMP(10);
      float s = tred4(asf4(p0) + asf4(p1));
      s += ror<4>(s); s += ror<8>(s);
      hv = rbf(xf + rbf(s));
      auto e = __builtin_amdgcn_permlane16_swap(asu(hv), asu(hv), false, false);
      auto e0 = __builtin_amdgcn_permlane32_swap(e[0], e[0], false, false);
      auto e1 = __builtin_amdgcn_permlane32_swap(e[1], e[1], false, false);
      STU((v4u{e0[0], e1[0], e0[1], e1[1]}) | tg, go, WS_BC);
      STAMP(9);
    }
    bar();
    bar();
    STAMP(14);
    {
      const v4f a0 = *(LAS v4f*)frp, a1 = *(LAS v4f*)(frp + 16); const float a2 = *(LAS float*)(frp + 32);
      const v4f a = a0 + a1;
      const float ov = hv + rbf(((a[0] + a[1]) + (a[2] + a[3])) + a2);
      if ((l & 12) == 0) *(GAS unsigned short*)outp = (unsigned short)pk(ov, ov);
      if (r == 0 && l == 0) __hip_atomic_store((GAS u32*)(ws + WS_EPOCH), ep + 1u, RLX);
    }
    STAMP(15);
    FIN(); return;
  }

  const __amdgpu_buffer_rsrc_t rws = __builtin_amdgcn_make_buffer_rsrc(ws, 0, WS_END, 0x00020000);
  const int tq = (l >> 2) & 3, tp = (l & 3) * 8;
  u32 one = 1u, tmask = TMASK;
  v4u tg;
  {
    const int rs = xcd >> 1, cbq = 2 * (t & 1) + (xcd & 1), gq_ = t >> 1;
    PINV(fl); PINV(one); PINV(tmask);
    if (w == 0) {
      LAS char* xp = L + L_X + (l >> 5) * 1152 + (l & 31) * 16;
      LAS char* xp2 = xp + ((l & 31) < 8 ? 1024 : 0);
      PINV(xp); PINV(xp2);
      LWAIT0(F_PROG, NX);
      v4u xs[3];
      xs[0] = *(LAS v4u*)xp; xs[1] = *(LAS v4u*)(xp + 512);
      const v4u t2 = *(LAS v4u*)xp2;
      xs[2] = (l & 31) < 8 ? t2 : v4u{0u, 0u, 0u, 0u};
      float s0 = 0.f, s1 = 0.f, q0 = 0.f, q1 = 0.f;
#pragma unroll
      for (int i = 0; i < 3; ++i) {
        s0 = dot2(xs[i][0], 0x3F803F80u, s0); q0 = dot2(xs[i][0], xs[i][0], q0); s1 = dot2(xs[i][1], 0x3F803F80u, s1); q1 = dot2(xs[i][1], xs[i][1], q1);
        s0 = dot2(xs[i][2], 0x3F803F80u, s0); q0 = dot2(xs[i][2], xs[i][2], q0); s1 = dot2(xs[i][3], 0x3F803F80u, s1); q1 = dot2(xs[i][3], xs[i][3], q1);
      }
      float mb, sd; ln_finish(s0 + s1, q0 + q1, mb, sd);
      *(LAS v2f*)(L + L_EP + 16 + (l >> 5) * 8) = v2f{mb, __builtin_amdgcn_rcpf(sd)};
      LINC(F_LN);
    }
    LWAIT(F_LN, 1);
    const v2u xa = *(LAS v2u*)(L + L_X + (l & 1) * 1152 + (rs * 144 + 16 * w + 4 * grp) * 2);
    tg = make_tag(*(LAS u32*)(L + L_EP));
    const v2f st = *(LAS v2f*)(L + L_EP + 16 + (l & 1) * 8);
    PINV(tg);
    v2u a;
#pragma unroll
    for (int k = 0; k < 2; ++k) { const u32 dlt = pk(lo16(xa[k]) - st[0], hi16(xa[k]) - st[0]); a[k] = pk(lo16(dlt) * st[1], hi16(dlt) * st[1]); }
    LAS char* qb = L + L_WQ + (2 * w + (grp >> 1)) * 1024 + (4 * (grp & 1) + tq) * 128 + tp;
    const int sw = ((tq >> 1) | (grp & 2)) << 5;
    LAS char* qb0 = qb + sw; LAS char* qb1 = qb + (32 ^ sw); LAS char* qb2 = qb + (64 ^ sw); LAS char* qb3 = qb + (96 ^ sw);
    LAS char* qw = L + L_QP + w * 512 + (l & 1) * 256 + (l & 6) * 32 + grp * 16;
    PINV(a); PINV(qb0); PINV(qb1); PINV(qb2); PINV(qb3); PINV(qw);
    STAMP(4);
    LWAIT0(F_PROG, 2 * w + 7);
    const v4f q0 = mfma16(trd(qb0), a, ZERO4), q1 = mfma16(trd(qb1), a, ZERO4), q2 = mfma16(trd(qb2), a, ZERO4), q3 = mfma16(trd(qb3), a, ZERO4);
    *(LAS v4f*)qw = sel4((l & 4) != 0, sel4((l & 2) != 0, q3, q2), sel4((l & 2) != 0, q1, q0));
    LINC(F_QP);
    STAMP(1);
    if (w == 8) {
      LAS char* qr = L + L_QP + (l & 31) * 16;
      int so = l < 32 ? grp * 4096 + (l & 15) * 16 : OOB, sb = WS_QG + gq_ * 8192 + rs * 1024 + cbq * 256;
      PINV(qr); PINV(so); PINS(sb);
      LWAIT0(F_QP, 9);
      const LAS v4f* q4p = (const LAS v4f*)qr;
      const v4f q4 = (((q4p[0] + q4p[32]) + (q4p[64] + q4p[96])) + ((q4p[128] + q4p[160]) + (q4p[192] + q4p[224]))) + q4p[256];
      const v4f q4r = asf4(__builtin_bit_cast(v4u, q4) + 0x200u);
      STU(ENC(q4r), so, sb);
      STAMP(2);
      int lo = l * 16, lb = WS_QG + g * 8192 + b * 4096;
      LAS char* qs = L + L_QS + l * 8;
      PINV(lo); PINS(lb); PINV(qs);
      LWAIT(F_PROG, QTRIG);
      v4u gq[4];
      GPOLL {
        CBAR();
#pragma unroll
        for (int i = 0; i < 4; ++i) gq[i] = LDU(lo + i * 1024, lb) ^ tg;
        if (ALLOK((bad(gq[0]) | bad(gq[1])) | (bad(gq[2]) | bad(gq[3])))) break;
        __builtin_amdgcn_s_sleep(PSLEEP);
      }
      const v4f qv = (asf4(gq[0]) + asf4(gq[1])) + (asf4(gq[2]) + asf4(gq[3]));
      *(LAS v2u*)qs = v2u{pk(qv[0], qv[1]), pk(qv[2], qv[3])};
      LINC(F_Q);
      STAMP(3);
      LWAIT(F_L, 4);
      const v4f ma = *(LAS v4f*)(L + L_WS + (l >> 5) * 32), la = *(LAS v4f*)(L + L_WS + (l >> 5) * 32 + 16);
      const float mc = fmaxf(fmaxf(ma[0], ma[1]), fmaxf(ma[2], ma[3]));
      const float lt = (la[0] * ex2((ma[0] - mc) * LOG2E) + la[1] * ex2((ma[1] - mc) * LOG2E)) + (la[2] * ex2((ma[2] - mc) * LOG2E) + la[3] * ex2((ma[3] - mc) * LOG2E));
      const v4f sq = {mc, lt, asf(__builtin_amdgcn_readlane(asu(mc), 32)), asf(__builtin_amdgcn_readlane(asu(lt), 32))};
      STU(ENC(sq), l == 0 ? 0 : OOB, WS_ST + g * 2048 + u * 128);
      STAMP(5);
      if (isW) {
        LWAIT(F_PROG, STRIG);
        v4u st;
        GPOLL {
          CBAR();
          st = LDU((l & 15) * 128, WS_ST + gw_ * 2048) ^ tg;
          if (ALLOK(bad(st))) break;
          __builtin_amdgcn_s_sleep(PSLEEP);
        }
        const float mj = asf(hh ? st[2] : st[0]), lj = asf(hh ? st[3] : st[1]);
        float M = fmaxf(mj, dpp<0x141>(mj)); M = fmaxf(M, dpp<0xB1>(M)); M = fmaxf(M, dpp<0x4E>(M));
        const float e = ex2((mj - M) * LOG2E);
        float Ls = e * lj; Ls += dpp<0x141>(Ls); Ls += dpp<0xB1>(Ls); Ls += dpp<0x4E>(Ls);
        *(LAS float*)(L + L_WN + (l & 7) * 8 + ((l >> 3) & 1) * 4) = e * __builtin_amdgcn_rcpf(Ls);
        LINC(F_WN);
        STAMP(6);
      }
    }
  }
  if (w >= 4 && w < 8) {
    const int v = w - 4;
    const int kx = (l & 15) * 256 + ((grp ^ (l & 15)) << 4);
    const int ka = L_T + ((2 * v + rot8) & 7) * 8192 + kx, kb = L_T + ((2 * v + 1 + rot8) & 7) * 8192 + kx;
    LAS char* qa = L + L_QS + (l & 1) * 256 + grp * 16;
    LAS char* pw = L + L_P + (l >> 5) * 512 + v * 128 + (l & 31) * 2;
    LAS char* sp = L + L_WS + (l >> 5) * 32 + v * 4;
    PINV(qa); PINV(pw); PINV(sp);
    LWAIT(F_PROG, N_K);
    v4u kf[4][4];
#pragma unroll
    for (int ds = 0; ds < 4; ++ds) {
      kf[ds][0] = *(LAS v4u*)(L + (ka ^ (ds << 6))); kf[ds][1] = *(LAS v4u*)(L + (ka ^ (ds << 6)) + 4096);
      kf[ds][2] = *(LAS v4u*)(L + (kb ^ (ds << 6))); kf[ds][3] = *(LAS v4u*)(L + (kb ^ (ds << 6)) + 4096);
    }
#pragma unroll
    for (int ds = 0; ds < 4; ++ds) { PINV(kf[ds][0]); PINV(kf[ds][1]); PINV(kf[ds][2]); PINV(kf[ds][3]); }
    LINC(F_QK);
    LWAIT0(F_Q, 1);
    v4u qf[4];
#pragma unroll
    for (int ds = 0; ds < 4; ++ds) qf[ds] = *(LAS v4u*)(qa + ds * 64);
    v4f s[4] = {ZERO4, ZERO4, ZERO4, ZERO4};
#pragma unroll
    for (int ds = 0; ds < 4; ++ds)
#pragma unroll
      for (int kt = 0; kt < 4; ++kt) s[kt] = mfma(qf[ds], kf[ds][kt], s[kt]);
    const bool odd = (grp & 1) != 0, up = l >= 32;
    const float a0 = odd ? s[1][0] : s[0][0], a1 = odd ? s[1][1] : s[0][1], b0 = odd ? s[3][0] : s[2][0], b1 = odd ? s[3][1] : s[2][1];
    const u32 r1 = pk(up ? a1 : a0, up ? b1 : b0);
    const u32 r2 = pk(lo16(r1) * 0.08838834764831845f, hi16(r1) * 0.08838834764831845f);
    const float fa = lo16(r2), fb = hi16(r2);
    const float mx = max16x(rowmax(fmaxf(fa, fb)));
    const float pa = ex2((fa - mx) * LOG2E), pb = ex2((fb - mx) * LOG2E);
    const u32 pp = pk(pa, pb);
    *(LAS unsigned short*)pw = (unsigned short)pp;
    *(LAS unsigned short*)(pw + 64) = (unsigned short)(pp >> 16);
    *(LAS float*)sp = mx;
    LINC(F_P);
    float ls = rowsum(pa + pb); ls = t16(ls, ls);
    *(LAS float*)(sp + 16) = ls;
    LINC(F_L);
    STAMP(5);
  }
  if (w < 4) {
    const int vrow = (8 * grp + tq) * 256 + (l & 1) * 8;
    const int c0 = (4 * w + ((l >> 1) & 1)) ^ (tq << 2) ^ ((2 * grp) & 3);
    LAS char* va00 = L + L_V + vrow + (c0 << 4); LAS char* va01 = L + L_V + vrow + 1024 + ((c0 ^ 1) << 4);
    LAS char* va10 = L + L_V + vrow + ((c0 ^ 2) << 4); LAS char* va11 = L + L_V + vrow + 1024 + ((c0 ^ 3) << 4);
    LAS char* pa = L + L_P + (l & 1) * 512 + grp * 16;
    LAS char* mp = L + L_WS + (l & 1) * 32;
    int so = (l & 12) == 0 ? (l & 1) * 8192 + (l & 2) * 32 + grp * 16 : OOB, sb = WS_OG + g * 16384 + u * 512 + w * 128;
    PINV(va00); PINV(va01); PINV(va10); PINV(va11); PINV(pa); PINV(mp); PINV(so); PINS(sb);
    v4u va[8], vb[8];
#define VREAD(i) do { va[i] = cat(trd(va00 + (i) * 8192), trd(va01 + (i) * 8192)); vb[i] = cat(trd(va10 + (i) * 8192), trd(va11 + (i) * 8192)); } while (0)
    LWAIT(F_PROG, N_K + 32);
    VREAD(0); VREAD(1); VREAD(2); VREAD(3);
    LWAIT0(F_P, 4);
    STAMP(5);
    v4u pf[8];
#pragma unroll
    for (int i = 0; i < 8; ++i) pf[i] = *(LAS v4u*)(pa + i * 64);
    const v4f mg = *(LAS v4f*)mp;
    const float mc = fmaxf(fmaxf(mg[0], mg[1]), fmaxf(mg[2], mg[3]));
    float f0 = ex2((mg[0] - mc) * LOG2E), f1 = ex2((mg[1] - mc) * LOG2E), f2 = ex2((mg[2] - mc) * LOG2E), f3 = ex2((mg[3] - mc) * LOG2E);
    const bool n1 = (l & 2) != 0;
    v4f o = sel4(n1, mfma(vb[1], pf[1], mfma(vb[0], pf[0], ZERO4)), mfma(va[1], pf[1], mfma(va[0], pf[0], ZERO4))) * f0;
    o += sel4(n1, mfma(vb[3], pf[3], mfma(vb[2], pf[2], ZERO4)), mfma(va[3], pf[3], mfma(va[2], pf[2], ZERO4))) * f1;
    PINV(o); PINV(f2); PINV(f3);
    LWAIT0(F_PROG, N_K + 48);
    VREAD(4); VREAD(5);
    o += sel4(n1, mfma(vb[5], pf[5], mfma(vb[4], pf[4], ZERO4)), mfma(va[5], pf[5], mfma(va[4], pf[4], ZERO4))) * f2;
    PINV(o);
    LWAIT0(F_PROG, N_K + 56);
    VREAD(6);
    v4f ta = mfma(va[6], pf[6], ZERO4), tb = mfma(vb[6], pf[6], ZERO4);
    LWAIT0(F_PROG, N_V);
    VREAD(7);
    o += sel4(n1, mfma(vb[7], pf[7], tb), mfma(va[7], pf[7], ta)) * f3;
    STU(ENC(o), so, sb);
    LINC(F_PV);
    STAMP(7);
  }
  const int t8 = tq * 128 + tp, sw8 = ((tq >> 1) | ((grp & 1) << 1)) << 5;
  if (isW) {
    if (w >= 4 && w < 8) {
      int lo = (l >> 3) * 512 + (l & 7) * 16, lb = WS_OG + hq * 8192 + (w - 4) * 128;
      LAS char* ob = L + L_OB + ((l >> 3) & 1) * 256 + (w - 4) * 64 + (l & 7) * 8 + grp * 2;
      PINV(lo); PINS(lb); PINV(ob);
      LWAIT(F_PV, 4);
      LWAIT0(F_WN, 1);
      const v2f wn = *(LAS v2f*)(L + L_WN + (l >> 3) * 8);
      v4u o0, o1;
      GPOLL {
        CBAR();
        o0 = LDU(lo, lb) ^ tg; o1 = LDU(lo, lb + 4096) ^ tg;
        if (ALLOK(bad(o0) | bad(o1))) break;
      }
      STAMP(8);
      const float r0 = tred4(asf4(o0) * wn[0]), r1 = tred4(asf4(o1) * wn[1]);
      const bool hi8 = (l & 8) != 0;
      const float val = (hi8 ? r1 : r0) + ror<8>(hi8 ? r0 : r1);
      *(LAS unsigned short*)ob = (unsigned short)pk(val, val);
      bar();
    } else if (w < 4) {
      LAS char* of = L + L_OB + (l & 1) * 256 + grp * 16;
      int so = (l & 15) < 6 ? (l & 1) * 2304 + (l & 6) * 32 + grp * 16 : OOB, sb = WS_RS + hq * 4608 + cb3 * 768 + w * 192;
      PINV(of); PINV(so); PINS(sb);
      LWAIT(F_PROG, N_W);
      v4u wf[3][4];
#pragma unroll
      for (int n = 0; n < 3; ++n) {
        LAS char* wp = L + L_T + (56 - 16 * ((3 * w + n) >> 2) + grp) * 1024 + t8 + ((((3 * w + n) & 3) << 5) ^ sw8);
#pragma unroll
        for (int ks = 0; ks < 4; ++ks) { wf[n][ks] = trd2(wp + (3 - ks) * 4096, 512); PINV(wf[n][ks]); }
      }
      bar();
      v4f y[3] = {ZERO4, ZERO4, ZERO4};
      v4u oq[4];
#pragma unroll
      for (int ks = 0; ks < 4; ++ks) oq[ks] = *(LAS v4u*)(of + ks * 64);
#pragma unroll
      for (int ks = 0; ks < 4; ++ks)
#pragma unroll
        for (int n = 0; n < 3; ++n) y[n] = mfma(wf[n][ks], oq[ks], y[n]);
      const v4f yv = sel4((l & 4) != 0, y[2], sel4((l & 2) != 0, y[1], y[0]));
      STU(ENC(yv), so, sb);
      STAMP(9);
    } else {
      bar();
    }
  }
  if (!isM) {
    if (isR) {
      int go = (l >> 2) * 4608 + r * 64 + (l & 3) * 16, gb = WS_H4 + w * 16 * 4608;
      LAS char* fr = L + L_FR + (4 * (l & 3) + grp) * 48 + w * 4;
      PINV(go); PINS(gb); PINV(fr);
      bar();
      v4u p;
      GPOLL {
        CBAR();
        p = LDU(go, gb) ^ tg;
        if (ALLOK(bad(p))) break;
      }
      STAMP(12);
      float s = tred4(asf4(p));
      s += ror<4>(s); s += ror<8>(s);
      *(LAS float*)fr = s;
      bar();
    }
    FIN(); return;
  }
  {
    LWAIT(F_PROG, N_T3);
    STAMP(11);
    v2u gf[4][2][2];
#pragma unroll
    for (int kb = 0; kb < 4; ++kb) {
      const int rg = 4 * w + kb;
      const int base = (rg < 18 ? L_T + (71 - 2 * rg) * 1024 : L_V + (2 * rg - 36) * 1024) + (4 * grp + tq) * 64 + tp, ms = rg < 18 ? -1024 : 1024;
#pragma unroll
      for (int mat = 0; mat < 2; ++mat)
#pragma unroll
        for (int nt = 0; nt < 2; ++nt) gf[kb][mat][nt] = trd(L + base + mat * ms + ((nt ^ (grp >> 1)) << 5));
    }
    const bool n1 = l >= 32;
    const int grow = (grp & 1) * 32 + (grp >> 1) * 16 + (l & 15);
    {
      const v2u ones = {0x3F803F80u, 0x3F803F80u};
      v4f cs[2][2];
#pragma unroll
      for (int mat = 0; mat < 2; ++mat)
#pragma unroll
        for (int nt = 0; nt < 2; ++nt) cs[mat][nt] = mfma16(ones, gf[3][mat][nt], mfma16(ones, gf[2][mat][nt], mfma16(ones, gf[1][mat][nt], mfma16(ones, gf[0][mat][nt], ZERO4))));
      if ((grp & 1) == 0) *(LAS v2f*)(L + L_SP + grow * 80 + w * 8) = v2f{n1 ? cs[0][1][0] : cs[0][0][0], n1 ? cs[1][1][0] : cs[1][0][0]};
    }
    int ho = ((l >> 2) & 1) * 2304 + (64 * w + 16 * (l & 3) + 4 * grp) * 4, bo = WS_BC + (xcd * 2 + (t & 1)) * BC_STRIDE;
    LAS char* gw = L + L_GP + grow * 80 + w * 8;
    LAS char* hr = L + L_HW + (l & 1) * 64 + grp * 16;
    int so = (l & 8) == 0 ? (l & 1) * 2304 + (l & 6) * 32 + grp * 16 : OOB, sb = WS_H4 + m * 4608 + w * 256;
    PINV(ho); PINS(bo); PINV(gw); PINV(hr); PINV(so); PINS(sb);
    LINC(F_CS);
    LAS char* d0 = L + L_T + (4 * w + grp) * 1024 + t8 + sw8; LAS char* d1 = L + L_T + (4 * w + grp) * 1024 + t8 + (32 ^ sw8);
    LAS char* d2 = L + L_T + (4 * w + grp) * 1024 + t8 + (64 ^ sw8); LAS char* d3 = L + L_T + (4 * w + grp) * 1024 + t8 + (96 ^ sw8);
    PINV(d0); PINV(d1); PINV(d2); PINV(d3);
    v4u hu;
    GPOLL {
      CBAR();
      hu = LDU(ho, bo);
      if (ALLOK(bad(hu ^ tg))) break;
    }
    STAMP(12);
    const v2u ha = {__builtin_amdgcn_perm(hu[1], hu[0], 0x07060302u), __builtin_amdgcn_perm(hu[3], hu[2], 0x07060302u)};
    float y[2][2];
#pragma unroll
    for (int mat = 0; mat < 2; ++mat)
#pragma unroll
      for (int nt = 0; nt < 2; ++nt) {
        const v4f a0 = mfma16(ha, gf[0][mat][nt], ZERO4), a1 = mfma16(ha, gf[1][mat][nt], ZERO4), a2 = mfma16(ha, gf[2][mat][nt], ZERO4), a3 = mfma16(ha, gf[3][mat][nt], ZERO4);
        y[mat][nt] = (a0[0] + a1[1]) + (a2[2] + a3[3]);
      }
    *(LAS v2f*)gw = v2f{n1 ? y[0][1] : y[0][0], n1 ? y[1][1] : y[1][0]};
    bar();
    STAMP(13);
    v4u df[4] = {trd2(d0, 512), trd2(d1, 512), trd2(d2, 512), trd2(d3, 512)};
    bar();
    const v4u da = *(LAS v4u*)hr;
    v4f d[4];
#pragma unroll
    for (int nt = 0; nt < 4; ++nt) d[nt] = mfma(df[nt], da, ZERO4);
    const v4f dv = sel4((l & 4) != 0, sel4((l & 2) != 0, d[3], d[2]), sel4((l & 2) != 0, d[1], d[0]));
    STU(ENC(dv), so, sb);
    STAMP(14);
  }
  FIN();
}

extern "C" void kernel_launch(void* const* d_in, const int* in_sizes, int n_in, void* d_out, int out_size, void* d_ws, size_t ws_size, hipStream_t stream) {
  (void)hipFuncSetAttribute((const void*)fused_decode, hipFuncAttributeMaxDynamicSharedMemorySize, L_END);
  fused_decode<<<256, 640, L_END, stream>>>((const char*)d_in[0], (char*)d_ws, (const char*)d_in[3], (const char*)d_in[1], (const char*)d_in[2], (const char*)d_in[4],
                                           (const char*)d_in[6], (const char*)d_in[7], (const char*)d_in[8], (char*)d_out);
}
```

```cpp
#include <hip/hip_runtime.h>
#include <stdint.h>

#define GAS __attribute__((address_space(1)))
#define LAS __attribute__((address_space(3)))
typedef unsigned u32;
typedef unsigned long long u64;
typedef unsigned v4u __attribute__((ext_vector_type(4)));
typedef unsigned v2u __attribute__((ext_vector_type(2)));
typedef float v4f __attribute__((ext_vector_type(4)));
typedef float v2f __attribute__((ext_vector_type(2)));
typedef __bf16 v8bf __attribute__((ext_vector_type(8)));
typedef __bf16 v2bf __attribute__((ext_vector_type(2)));
typedef short v4s __attribute__((ext_vector_type(4)));

constexpr int WIN = 32;
constexpr int LW = 20;
constexpr int TOFF = 0;
constexpr int DQ = 62;
constexpr int QD = 100;
constexpr int GATE = 2;
constexpr int SWIN = 14;
constexpr int OWIN = 33;
constexpr int BCU = 128;
constexpr int HSL = 2;
constexpr int NB3 = 6;
constexpr int NR7 = 4;
constexpr int DS = 34;
constexpr int TS0 = 490;
constexpr int FD = 240;
constexpr int GSHIFT = 2;
constexpr int PSLEEP = 4;
constexpr int CODE_LAST = 20736;

constexpr int WS_EPOCH = 0, WS_ST = 4096, WS_QG = WS_ST + 32768, WS_OG = WS_QG + 131072, WS_RS = WS_OG + 262144, WS_BC = WS_RS + 32 * 4608, BC_STRIDE = 72 * BCU,
              WS_H4 = WS_BC + 16 * BC_STRIDE, WS_END = WS_H4 + 144 * 4608;
constexpr int L_T = 0, L_WQ = 65536, L_GP = 73728, L_SP = L_GP + 5120, L_X = 83968, L_JUNK = 87040, L_V = 88064, L_FLAG = L_V + 65536, L_EP = L_FLAG + 64,
              L_QP = L_EP + 256, L_QS = L_QP + 4608, L_P = L_QS + 512, L_WS = L_P + 1024, L_OB = L_WS + 64, L_HW = L_OB + 512, L_FR = L_HW + 128, L_WN = L_FR + 768, L_END = L_WN + 64;
static_assert(L_END <= 163840 && L_SP + 5120 <= L_X && L_WQ + 18432 <= L_X, "LDS");
constexpr int F_PROG = 0, F_QP = 1, F_Q = 2, F_QK = 3, F_P = 4, F_PV = 5, F_WN = 6, F_LN = 7, F_L = 8, F_CS = 9, F_T = 11;
constexpr int NX = 5, N0 = 23, N_K = N0 + 64, N_V = N_K + 64;
constexpr int SC1 = 16, NT = 2;
constexpr u32 GSPIN = 1u << 21, LSPIN = 1u << 26;
constexpr int OOB = 0x7ffffff0;
constexpr u32 TMASK = 0x3FFu, QMASK = 0xFFu;

#define CBAR() asm volatile("" ::: "memory")
#define PINV(x) asm volatile("" : "+v"(x))
#define PINS(x) asm volatile("" : "+s"(x))
#define RETRY() asm volatile("s_nop 0")
#define GPOLL _Pragma("nounroll") for (u32 sp_ = 0; sp_ < GSPIN; ++sp_)
#define RLX __ATOMIC_RELAXED, __HIP_MEMORY_SCOPE_AGENT
__device__ __forceinline__ void bar() { asm volatile("s_waitcnt lgkmcnt(0)\n\ts_barrier" ::: "memory"); __builtin_amdgcn_sched_barrier(0); }
__device__ __forceinline__ u32 pk(float lo, float hi) { return __builtin_bit_cast(u32, __builtin_convertvector(v2f{lo, hi}, v2bf)); }
__device__ __forceinline__ float asf(u32 u) { return __builtin_bit_cast(float, u); }
__device__ __forceinline__ u32 asu(float f) { return __builtin_bit_cast(u32, f); }
__device__ __forceinline__ v4f asf4(v4u u) { return __builtin_bit_cast(v4f, u); }
__device__ __forceinline__ float lo16(u32 u) { return asf(u << 16); }
__device__ __forceinline__ float hi16(u32 u) { return asf(u & 0xffff0000u); }
__device__ __forceinline__ float rbf(float f) { return lo16(pk(f, f)); }
__device__ __forceinline__ v4f mfma(v4u a, v4u b, v4f c) { return __builtin_amdgcn_mfma_f32_16x16x32_bf16(__builtin_bit_cast(v8bf, a), __builtin_bit_cast(v8bf, b), c, 0, 0, 0); }
__device__ __forceinline__ v4f mfma16(v2u a, v2u b, v4f c) { return __builtin_amdgcn_mfma_f32_16x16x16bf16_1k(__builtin_bit_cast(v4s, a), __builtin_bit_cast(v4s, b), c, 0, 0, 0); }
__device__ __forceinline__ float dot2(u32 a, u32 b, float c) { return __builtin_amdgcn_fdot2_f32_bf16(__builtin_bit_cast(v2bf, a), __builtin_bit_cast(v2bf, b), c, false); }
template <int C> __device__ __forceinline__ float dpp(float x) { return asf(__builtin_amdgcn_mov_dpp(asu(x), C, 0xf, 0xf, true)); }
template <int N> __device__ __forceinline__ float ror(float x) { return asf(__builtin_amdgcn_mov_dpp(asu(x), 0x120 + N, 0xf, 0xf, true)); }
__device__ __forceinline__ float t16(float x, float y) { auto s = __builtin_amdgcn_permlane16_swap(asu(x), asu(y), false, false); return asf(s[0]) + asf(s[1]); }
__device__ __forceinline__ float t32(float x, float y) { auto s = __builtin_amdgcn_permlane32_swap(asu(x), asu(y), false, false); return asf(s[0]) + asf(s[1]); }
__device__ __forceinline__ float tred4(v4f v) { return t16(t32(v[0], v[2]), t32(v[1], v[3])); }
__device__ __forceinline__ float max16x(float x) { auto s = __builtin_amdgcn_permlane16_swap(asu(x), asu(x), false, false); return fmaxf(asf(s[0]), asf(s[1])); }
__device__ __forceinline__ float rowsum(float x) { x += ror<8>(x); x += ror<4>(x); x += ror<2>(x); x += ror<1>(x); return x; }
__device__ __forceinline__ float rowmax(float x) { x = fmaxf(x, ror<8>(x)); x = fmaxf(x, ror<4>(x)); x = fmaxf(x, ror<2>(x)); x = fmaxf(x, ror<1>(x)); return x; }
__device__ __forceinline__ float ex2(float x) { return __builtin_amdgcn_exp2f(x); }
__device__ __forceinline__ v2u trd(LAS char* p) { return __builtin_bit_cast(v2u, __builtin_amdgcn_ds_read_tr16_b64_v4i16((LAS v4s*)p)); }
__device__ __forceinline__ v4u cat(v2u a, v2u b) { return v4u{a[0], a[1], b[0], b[1]}; }
__device__ __forceinline__ v4u trd2(LAS char* p, int d) { return cat(trd(p), trd(p + d)); }
__device__ __forceinline__ v4f fma4(v4f a, float f, v4f c) { return __builtin_elementwise_fma(a, v4f{f, f, f, f}, c); }
__device__ __forceinline__ v4f sel4(bool c, v4f a, v4f b) { return v4f{c ? a[0] : b[0], c ? a[1] : b[1], c ? a[2] : b[2], c ? a[3] : b[3]}; }
__device__ __forceinline__ u32 bfi(u32 mask, u32 a, u32 b) { return (mask & a) | (~mask & b); }
#define ENC(v) (v4u{bfi(tmask, tg[0], asu((v)[0])), bfi(tmask, tg[1], asu((v)[1])), bfi(tmask, tg[2], asu((v)[2])), bfi(tmask, tg[3], asu((v)[3]))})
__device__ __forceinline__ u32 bad(v4u y) { return (y[0] | y[1]) | (y[2] | y[3]); }
#define ALLOK(x) (__builtin_amdgcn_ballot_w64(((x) & TMASK) != 0u) == 0ull)
constexpr float LOG2E = 1.4426950408889634f;
#define ZERO4 (v4f{0.f, 0.f, 0.f, 0.f})

__device__ __forceinline__ void ln_tail(float z, float& mean_bf, float& sd_bf) {
  auto e = __builtin_amdgcn_permlane16_swap(asu(z), asu(z), false, false);
  const float m = asf(e[0]) * (1.f / 576.f);
  const float var = fmaxf(asf(e[1]) * (1.f / 576.f) - m * m, 0.f);
  mean_bf = rbf(m);
  sd_bf = rbf(__builtin_amdgcn_sqrtf(rbf(rbf(var) + 1e-5f)));
}
__device__ __forceinline__ void ln_finish(float s, float ss, float& mean_bf, float& sd_bf) { ln_tail(rowsum(t16(s, ss)), mean_bf, sd_bf); }
#define WAITV(A) asm volatile("s_waitcnt vmcnt(%0)" :: "i"(A) : "memory")
#define PUB(val) asm volatile("ds_write_b32 %0, %1" :: "v"(fl), "v"(val) : "memory")
#define WPUB(A, val) asm volatile("s_waitcnt vmcnt(%0)\n\tds_write_b32 %1, %2" :: "i"(A), "v"(fl), "v"(val) : "memory")
template <int F, int SLEEP> __device__ __forceinline__ void lwait(u32 fl, u32 n) {
#pragma nounroll
  for (u32 sp = 0; sp < LSPIN; ++sp) {
    u32 v; asm volatile("ds_read_b32 %0, %1 offset:%2\n\ts_waitcnt lgkmcnt(0)" : "=v"(v) : "v"(fl), "i"(4 * F) : "memory");
    if ((u32)__builtin_amdgcn_readfirstlane(v) >= n) break;
    if (SLEEP) __builtin_amdgcn_s_sleep(SLEEP); else RETRY();
  }
  __builtin_amdgcn_sched_barrier(0);
}
template <int F> __device__ __forceinline__ u32 lpeek(u32 fl) { u32 v; asm volatile("ds_read_b32 %0, %1 offset:%2\n\ts_waitcnt lgkmcnt(0)" : "=v"(v) : "v"(fl), "i"(4 * F) : "memory"); return __builtin_amdgcn_readfirstlane(v); }
#define LWAIT(F, n) lwait<F, 1>(fl, (u32)(n))
#define LWAIT0(F, n) lwait<F, 0>(fl, (u32)(n))
#define LINC(F) asm volatile("s_mov_b64 exec, 1\n\tds_add_u32 %0, %1 offset:%2\n\ts_mov_b64 exec, -1" :: "v"(fl), "v"(one), "i"(4 * (F)) : "memory")
#define DMA0(src, dst, imm) __builtin_amdgcn_global_load_lds((const GAS u32*)(src), (LAS u32*)(L + (dst)), 16, imm, 0)
#define RSRC(p) __builtin_amdgcn_make_buffer_rsrc((void*)(p), 0, 0x7fffffff, 0x00020000)
#define BDMA(rs, vo, so, dst, imm) __builtin_amdgcn_raw_ptr_buffer_load_lds((rs), (LAS void*)(L + (dst)), 16, (vo), (so), (imm), NT)
#define BDMA3(rs, v, so, dst) do { BDMA(rs, v[0], so, dst, 0); BDMA(rs, v[1], so, dst, 1024); BDMA(rs, v[2], so, dst, 2048); } while (0)
#define LDU(off, soff) __builtin_amdgcn_raw_buffer_load_b128(rws, (off), (soff), SC1)
#define STU(v, off, soff) do { __builtin_amdgcn_raw_buffer_store_b128((v), rws, (off), (soff), SC1); __builtin_amdgcn_sched_barrier(0); asm volatile("s_nop 1" ::: "memory"); __builtin_amdgcn_sched_barrier(0); } while (0)
__device__ __forceinline__ v4u make_tag(u32 ep) { const u32 t = (ep + 1u) * 0x2F5u; return v4u{t & TMASK, (t + 341u) & TMASK, (t + 682u) & TMASK, (t + 1023u) & TMASK}; }

__global__ void __launch_bounds__(704, 3) fused_decode(const char* __restrict__ x, char* __restrict__ ws, const char* __restrict__ wq, const char* __restrict__ kc,
                                                    const char* __restrict__ vc, const char* __restrict__ wo, const char* __restrict__ w1,
                                                    const char* __restrict__ w3, const char* __restrict__ w2, char* __restrict__ out) {
  extern __shared__ __attribute__((aligned(16))) char lds_[];
  LAS char* L = (LAS char*)lds_;
  u64 pc0; asm volatile("s_getpc_b64 %0" : "=s"(pc0));
  const int l = threadIdx.x & 63, wid = __builtin_amdgcn_readfirstlane(threadIdx.x >> 6), w = wid - 1;
  const int c = blockIdx.x, xcd = c & 7, t = c >> 3, g = xcd * 2 + (t >> 4), u = t & 15, b = u >> 3, j = u & 7;
  const int grp = l >> 4;
  const bool isM = u < 9, isW = u >= 9 && u < 15;
  const int m = g * 9 + u, r = g * 6 + (u - 9), hh = u >= 12 ? 1 : 0, cb3 = u - 9 - 3 * hh;
  const bool isR = isW && r < 72;
  const int gw_ = (g + GSHIFT) & 15, hq = gw_ * 2 + hh;
  const int rot = (c + (c >> 4)) & 15, rot8 = rot >> 1;
  u32 fl = L_FLAG;
  u32 tz = 0;
  u32 now = 0;
#define DUE(tk) do { _Pragma("nounroll") while (now < (u32)(tk)) now = (u32)__builtin_amdgcn_s_memrealtime() - tz; } while (0)
#define DUES(tk) do { _Pragma("nounroll") while (now < (u32)(tk)) { __builtin_amdgcn_s_sleep(1); now = (u32)__builtin_amdgcn_s_memrealtime() - tz; } } while (0)
  const u32 p8r = l >> 3, p8c = ((l & 7) ^ ((l >> 3) & 2)) << 4;
  if (threadIdx.x < 64) {
    asm volatile("ds_write_b32 %0, %1" :: "v"(L_FLAG + (l & 15) * 4), "v"(0) : "memory");
    DMA0((pc0 & ~255ull) + (u32)((t * 1024 < CODE_LAST ? t * 1024 : CODE_LAST) + l * 16), L_JUNK, 0);
  }
  u32 xo = l * 16, xo2 = (l & 15) * 16;
  u32 qo0 = (xcd >> 1) * 144 * 8192 + t * 256 + (xcd & 1) * 128 + p8r * 8192 + p8c, qo1 = (qo0 ^ 64) + 65536 - 1024;
  const u32 reg = ((b * 16 + g) * 8192 + 6144 + j * 256) * 256;
  u32 kv0 = reg + grp * 256 + (((l & 15) ^ grp) << 4), kv1 = kv0 ^ 64, kv2 = kv0 ^ 128, kv3 = kv0 ^ 192;
  int rot12 = rot * 4096;
  PINV(xo); PINV(xo2); PINV(qo0); PINV(qo1); PINV(kv0); PINV(kv1); PINV(kv2); PINV(kv3); PINS(rot12);
  asm volatile("" :: "s"(x), "s"(ws), "s"(wq), "s"(kc), "s"(vc), "s"(wo), "s"(w1), "s"(w3), "s"(w2), "s"(out));
  asm volatile("s_cmp_eq_u32 %0, 0\n\ts_cbranch_scc1 1f\n\ts_barrier\n1:" :: "s"(wid) : "memory", "scc");

  if (__builtin_expect(wid == 0, 1)) {
    DMA0((const GAS char*)x + xo, L_X, 0); DMA0((const GAS char*)x + xo, L_X, 1024); DMA0((const GAS char*)x + xo2, L_X, 2048);
    __builtin_amdgcn_global_load_lds((const GAS u32*)(ws + WS_EPOCH), (LAS u32*)(L + L_EP), 4, 0, SC1);
    tz = (u32)__builtin_amdgcn_s_memrealtime();
    {
      const __amdgpu_buffer_rsrc_t rq = RSRC(wq);
#pragma unroll
      for (int i = 0; i < 9; ++i) { BDMA(rq, qo0, i * 131072, L_WQ + i * 2048, 0); BDMA(rq, qo1, i * 131072, L_WQ + i * 2048, 1024); }
    }
    const __amdgpu_buffer_rsrc_t rk = RSRC(kc);
#define KGRP(gi) do { const u32 go_ = ((gi) * 4096 + rot12) & 0xF000; \
      BDMA(rk, kv0, go_, L_T + go_, 0); BDMA(rk, kv1, go_, L_T + go_, 1024); BDMA(rk, kv2, go_, L_T + go_, 2048); BDMA(rk, kv3, go_, L_T + go_, 3072); } while (0)
    KGRP(0);
    __builtin_amdgcn_s_setprio(3);
    bar();
    WPUB(N0 + 4 - NX, NX);
#define PACE(n) do { const int ld_ = (n) - LW; if (ld_ == N0 - 8 || ld_ == N0 || ld_ == N_K || ld_ == N_K + 32) WPUB(LW, ld_); else WAITV(WIN - 4); } while (0)
#pragma unroll
    for (int gi = 1; gi < 16; ++gi) { DUE(TOFF + gi * DQ / 4); PACE(N0 + gi * 4); KGRP(gi); }
    const __amdgpu_buffer_rsrc_t rv = RSRC(vc);
    const u32 vv0 = reg + grp * 256 + (((l & 15) ^ (grp << 2)) << 4);
#pragma unroll
    for (int gi = 0; gi < 16; ++gi) {
      DUE(TOFF + (16 + gi) * DQ / 4);
      PACE(N_K + gi * 4);
      const u32 so = (((gi + 2 * rot8) & 15) ^ (rot & 1)) * 4096; const int d = L_V + (gi ^ (rot & 1)) * 4096;
      BDMA(rv, vv0, so, d, 0); BDMA(rv, vv0 ^ 16, so, d, 1024); BDMA(rv, vv0 ^ 32, so, d, 2048); BDMA(rv, vv0 ^ 48, so, d, 3072);
    }
    const __amdgpu_buffer_rsrc_t rws = __builtin_amdgcn_make_buffer_rsrc(ws, 0, WS_END, 0x00020000);
    PUB(N_K + 33);
    WPUB(16, N_V - 16); WPUB(8, N_V - 8); WPUB(0, N_V);
    if (isM) {
      int xso = ((l >> 5) * 36 + ((l & 31) >> 2)) * BCU + (l & 3) * 16, xso4 = ((l >> 5) * 36 + 32 + ((l & 15) >> 2)) * BCU + (l & 3) * 16, bo = WS_BC + (xcd * 2 + (t & 1)) * BC_STRIDE;
      LAS char* gpp = L + L_GP + l * 80; LAS char* hwp = L + L_HW + l * 2;
      v4u tg = make_tag(*(LAS u32*)(L + L_EP));
      LWAIT(F_CS, 9);
      v2f cs2;
      {
        const LAS v4f* sp = (const LAS v4f*)(L + L_SP + (l & 31) * 80);
        const v4f s = (sp[0] + sp[1]) + (sp[2] + sp[3]);
        const v2f s8 = *(const LAS v2f*)(sp + 4);
        cs2 = v2f{s[0] + s[2] + s8[0], s[1] + s[3] + s8[1]};
      }
      PINV(cs2); PINV(tg); PINV(xso); PINV(xso4); PINS(bo); PINV(gpp); PINV(hwp);
      v4u hx[5];
      GPOLL {
        CBAR();
#pragma unroll
        for (int i = 0; i < 4; ++i) hx[i] = LDU(xso + i * 8 * BCU, bo) ^ tg;
        hx[4] = LDU(xso4, bo) ^ tg;
        if (ALLOK((bad(hx[0]) | bad(hx[1])) | (bad(hx[2]) | bad(hx[3])) | bad(hx[4]))) break;
        __builtin_amdgcn_s_sleep(HSL);
      }
      if (l & 16) hx[4] = v4u{0u, 0u, 0u, 0u};
      v4f s4 = asf4(hx[0]), q4 = s4 * s4;
#pragma unroll
      for (int i = 1; i < 5; ++i) { const v4f v = asf4(hx[i]); s4 += v; q4 += v * v; }
      float z = rowsum(t16((s4[0] + s4[1]) + (s4[2] + s4[3]), (q4[0] + q4[1]) + (q4[2] + q4[3])));
      PINV(z);
      bar();
      {
        const LAS v4f* gp4 = (const LAS v4f*)gpp;
        const v4f g0 = gp4[0], g1 = gp4[1], g2 = gp4[2], g3 = gp4[3]; const v2f g4 = *(const LAS v2f*)(gp4 + 4);
        __builtin_amdgcn_sched_barrier(0);
        float mb, sd; ln_tail(z, mb, sd);
        float rsd = __builtin_amdgcn_rcpf(sd);
        v2f off = cs2 * mb;
        PINV(rsd); PINV(off);
        __builtin_amdgcn_sched_barrier(0);
        const v4f s = (g0 + g1) + (g2 + g3);
        const v2f s8 = g4 - off;
        const u32 gu = pk((s[0] + s[2] + s8[0]) * rsd, (s[1] + s[3] + s8[1]) * rsd);
        const float gg = lo16(gu), uu = hi16(gu);
        const float hd = rbf(gg * __builtin_amdgcn_rcpf(1.f + ex2(-gg * LOG2E))) * uu;
        *(LAS unsigned short*)hwp = (unsigned short)pk(hd, hd);
      }
      bar();
      return;
    }
    if (!isR) return;
    const u32 ep = *(LAS u32*)(L + L_EP);
    v4u tg = make_tag(ep);
    float xf = lo16(*(LAS unsigned short*)(L + L_X + r * 32 + (l & 3) * 8 + grp * 2));
    int go = (l >> 2) * 4608 + r * 64 + (l & 3) * 16;
    LAS char* frp = L + L_FR + (4 * (l & 3) + grp) * 48;
    GAS char* outp = (GAS char*)out + r * 32 + (l & 3) * 8 + grp * 2;
    int bco = (l >> 2) * BC_STRIDE + r * BCU + (l & 3) * 16;
    PINV(tg); PINV(xf); PINV(go); PINV(frp); PINV(outp); PINV(bco);
    { v4u a0 = LDU(go, WS_RS), a1 = LDU(go, WS_RS + 16 * 4608); PINV(a0); PINV(a1); }
    bar();
    float hv;
    {
      v4u p0, p1;
      GPOLL {
        CBAR();
        p0 = LDU(go, WS_RS) ^ tg; p1 = LDU(go, WS_RS + 16 * 4608) ^ tg;
        if (ALLOK(bad(p0) | bad(p1))) break;
        RETRY();
      }
      float s = tred4(asf4(p0) + asf4(p1));
      s += ror<8>(s); s += ror<4>(s);
      hv = rbf(xf + rbf(s));
      auto e = __builtin_amdgcn_permlane16_swap(asu(hv), asu(hv), false, false);
      auto e0 = __builtin_amdgcn_permlane32_swap(e[0], e[0], false, false);
      auto e1 = __builtin_amdgcn_permlane32_swap(e[1], e[1], false, false);
      STU((v4u{e0[0], e1[0], e0[1], e1[1]}) | tg, bco, WS_BC);
    }
    if (r == 0 && l == 0) __hip_atomic_store((GAS u32*)(ws + WS_EPOCH), ep + 1u, RLX);
    tz = (u32)__builtin_amdgcn_s_memrealtime(); now = 0; DUES(FD);
    bar();
    bar();
    {
      const v4f a0 = *(LAS v4f*)frp, a1 = *(LAS v4f*)(frp + 16); const float a2 = *(LAS float*)(frp + 32);
      const v4f a = a0 + a1;
      const float ov = hv + rbf(((a[0] + a[1]) + (a[2] + a[3])) + a2);
      if ((l & 12) == 0) *(GAS unsigned short*)outp = (unsigned short)pk(ov, ov);
    }
    return;
  }

  if (wid == 10) {
    __builtin_amdgcn_s_setprio(2);
    tz = (u32)__builtin_amdgcn_s_memrealtime();
    const u32 o2 = p8r * 1152 + p8c;
    const u32 e2[3] = {o2 + 1792, o2 + 896, o2}, f2[3] = {(o2 ^ 64) + 1792, (o2 ^ 64) + 896, o2 ^ 64};
#define TPUB(A, val) asm volatile("s_waitcnt vmcnt(%0)\n\tds_write_b32 %1, %2 offset:%3" :: "i"(A), "v"(fl), "v"(val), "i"(4 * F_T) : "memory")
    if (isM) {
      const u32 o13 = (l >> 2) * 9216 + (((l & 3) ^ ((l >> 4) & 2)) << 4);
      const __amdgpu_buffer_rsrc_t r1 = RSRC(w1 + m * 64), r3 = RSRC(w3 + (m * 64 - 1024)), r2 = RSRC(w2 + (m * 36864 - 1792));
      LWAIT(F_QP, 9); LWAIT(F_QK, 4);
#pragma unroll
      for (int rg = 0; rg < 36; ++rg) {
        if (rg == 0) LWAIT(F_PROG, N_K + 33);
        WAITV(SWIN);
        DUE(TS0 + rg * DS / 4);
        BDMA(r1, o13, rg * 147456, L_T + rg * 2048, 0); BDMA(r3, o13, rg * 147456, L_T + rg * 2048, 1024);
      }
      LWAIT0(F_PV, 4);
#pragma unroll
      for (int gi = 0; gi < 12; ++gi) {
        const int k = gi / 3, lg = gi % 3;
        if (k & 1) BDMA3(r2, f2, k * 9216 + lg * 384, L_V + gi * 3072); else BDMA3(r2, e2, k * 9216 + lg * 384, L_V + gi * 3072);
      }
      TPUB(36, 1);
      WAITV(0);
    } else if (isW) {
      const __amdgpu_buffer_rsrc_t ro = RSRC(wo + (hq * 128 * 1152 + cb3 * 384 - 1792));
      LWAIT(F_QP, 9); LWAIT(F_QK, 4);
      LWAIT(F_PROG, N_K + 33);
#pragma unroll
      for (int gi = 0; gi < 16; ++gi) {
        WAITV(OWIN - 3);
        if (gi & 1) BDMA3(ro, f2, gi * 9216, L_T + gi * 3072); else BDMA3(ro, e2, gi * 9216, L_T + gi * 3072);
      }
      TPUB(24, 2); TPUB(12, 3); TPUB(0, 4);
    } else {
      const char* wm = (t & 16) ? w3 : w1;
      const u32 c3 = (3u - (u32)((u64)wm >> 7) - 9u * xcd) & 7u;
      const __amdgpu_buffer_rsrc_t rp = RSRC(wm + (9 * xcd + c3) * 128);
      const u32 po = l * 9216;
#define PFI(i, imm) __builtin_amdgcn_raw_ptr_buffer_load_lds(rp, (LAS void*)(L + L_JUNK - (imm)), 4, po, (8 - (i)) * 589824, (imm), 0)
      LWAIT(F_PROG, N_K + 33);
      PFI(0, 0);
      LWAIT0(F_PROG, N_V - 8);
      PFI(1, 0);
      LWAIT0(F_PV, 4);
      if (c3 == 0u) { PFI(0, 1024); PFI(1, 1024); }
#pragma unroll
      for (int i = 2; i < NB3; ++i) { PFI(i, 0); if (c3 == 0u) PFI(i, 1024); }
      {
        const __amdgpu_buffer_rsrc_t rr = RSRC(wm + xcd * 1152);
        const u32 lr = l < 63 ? l : 62, pr = (lr / 9) * 9216 + (lr % 9) * 128;
#pragma unroll
        for (int i = 0; i < NR7; ++i) __builtin_amdgcn_raw_ptr_buffer_load_lds(rr, (LAS void*)(L + L_JUNK), 4, pr, (569 - 7 * i) * 9216, 0, 0);
      }
      WAITV(0);
    }
    return;
  }
  const __amdgpu_buffer_rsrc_t rws = __builtin_amdgcn_make_buffer_rsrc(ws, 0, WS_END, 0x00020000);
  const int tq = (l >> 2) & 3, tp = (l & 3) * 8;
  u32 one = 1u, tmask = TMASK;
  v4u tg;
  {
    const int rs = xcd >> 1, cbq = 2 * (t & 1) + (xcd & 1), gq_ = t >> 1;
    PINV(fl); PINV(one); PINV(tmask);
    if (w == 1) {
      const char* tb = (l & 8) ? ((l & 4) ? wo : w2) : ((l & 4) ? w3 : w1);
      tb += ((l & 3) == 3 ? 0 : (l & 3)) * 2097152;
      if (l & 48) tb = vc + reg;
      u32 keep;
      asm volatile("s_mov_b32 %0, m0\n\ts_mov_b32 m0, %2\n\ts_nop 0\n\tglobal_load_lds_dword %1, off\n\ts_mov_b32 m0, %0" : "=&s"(keep) : "v"(tb), "s"(L_JUNK) : "memory");
    }
    if (w == 0) {
      LAS char* xp = L + L_X + (l >> 5) * 1152 + (l & 31) * 16;
      LAS char* xp2 = xp + ((l & 31) < 8 ? 1024 : 0);
      PINV(xp); PINV(xp2);
      LWAIT0(F_PROG, NX);
      v4u xs[3];
      xs[0] = *(LAS v4u*)xp; xs[1] = *(LAS v4u*)(xp + 512);
      const v4u t2 = *(LAS v4u*)xp2;
      xs[2] = (l & 31) < 8 ? t2 : v4u{0u, 0u, 0u, 0u};
      float s0 = 0.f, s1 = 0.f, q0 = 0.f, q1 = 0.f;
#pragma unroll
      for (int i = 0; i < 3; ++i) {
        s0 = dot2(xs[i][0], 0x3F803F80u, s0); q0 = dot2(xs[i][0], xs[i][0], q0); s1 = dot2(xs[i][1], 0x3F803F80u, s1); q1 = dot2(xs[i][1], xs[i][1], q1);
        s0 = dot2(xs[i][2], 0x3F803F80u, s0); q0 = dot2(xs[i][2], xs[i][2], q0); s1 = dot2(xs[i][3], 0x3F803F80u, s1); q1 = dot2(xs[i][3], xs[i][3], q1);
      }
      float mb, sd; ln_finish(s0 + s1, q0 + q1, mb, sd);
      *(LAS v2f*)(L + L_EP + 16 + (l >> 5) * 8) = v2f{mb, __builtin_amdgcn_rcpf(sd)};
      LINC(F_LN);
    }
    LWAIT(F_LN, 1);
    const v2u xa = *(LAS v2u*)(L + L_X + (l & 1) * 1152 + (rs * 144 + 16 * w + 4 * grp) * 2);
    tg = make_tag(*(LAS u32*)(L + L_EP));
    const v2f st = *(LAS v2f*)(L + L_EP + 16 + (l & 1) * 8);
    PINV(tg);
    v2u a;
#pragma unroll
    for (int k = 0; k < 2; ++k) { const u32 dlt = pk(lo16(xa[k]) - st[0], hi16(xa[k]) - st[0]); a[k] = pk(lo16(dlt) * st[1], hi16(dlt) * st[1]); }
    LAS char* qb = L + L_WQ + (2 * w + (grp >> 1)) * 1024 + (4 * (grp & 1) + tq) * 128 + tp;
    const int sw = ((tq >> 1) | (grp & 2)) << 5;
    LAS char* qb0 = qb + sw; LAS char* qb1 = qb + (32 ^ sw); LAS char* qb2 = qb + (64 ^ sw); LAS char* qb3 = qb + (96 ^ sw);
    LAS char* qw = L + L_QP + w * 512 + (l & 1) * 256 + (l & 6) * 32 + grp * 16;
    PINV(a); PINV(qb0); PINV(qb1); PINV(qb2); PINV(qb3); PINV(qw);
    LWAIT0(F_PROG, w < 5 ? N0 - 8 : N0);
    const v4f q0 = mfma16(trd(qb0), a, ZERO4), q1 = mfma16(trd(qb1), a, ZERO4), q2 = mfma16(trd(qb2), a, ZERO4), q3 = mfma16(trd(qb3), a, ZERO4);
    *(LAS v4f*)qw = sel4((l & 4) != 0, sel4((l & 2) != 0, q3, q2), sel4((l & 2) != 0, q1, q0));
    LINC(F_QP);
    if (w == 1) WAITV(0);
    if (w == 8) {
      LAS char* qr = L + L_QP + (l & 31) * 16;
      int so = l < 32 ? grp * 4096 + (l & 15) * 16 : OOB, sb = WS_QG + gq_ * 8192 + rs * 1024 + cbq * 256;
      PINV(qr); PINV(so); PINS(sb);
      LWAIT0(F_QP, 9);
      const LAS v4f* q4p = (const LAS v4f*)qr;
      const v4f q4 = (((q4p[0] + q4p[32]) + (q4p[64] + q4p[96])) + ((q4p[128] + q4p[160]) + (q4p[192] + q4p[224]))) + q4p[256];
      const v4u tq8 = tg & QMASK;
      STU(((__builtin_bit_cast(v4u, q4) + (QMASK + 1u) / 2u) & ~QMASK) | tq8, so, sb);
      int lo = l * 16, lb = WS_QG + g * 8192 + b * 4096;
      LAS char* qs = L + L_QS + l * 8;
      PINV(lo); PINS(lb); PINV(qs);
      tz = (u32)__builtin_amdgcn_s_memrealtime();
      DUES(QD);
      v4u gq[4];
      GPOLL {
        CBAR();
#pragma unroll
        for (int i = 0; i < 4; ++i) gq[i] = LDU(lo + i * 1024, lb) ^ tq8;
        if (ALLOK(((bad(gq[0]) | bad(gq[1])) | (bad(gq[2]) | bad(gq[3]))) & QMASK)) break;
        __builtin_amdgcn_s_sleep(PSLEEP);
      }
      const v4f qv = (asf4(gq[0]) + asf4(gq[1])) + (asf4(gq[2]) + asf4(gq[3]));
      *(LAS v2u*)qs = v2u{pk(qv[0], qv[1]), pk(qv[2], qv[3])};
      LINC(F_Q);
      LWAIT(F_L, 4);
      const v4f ma = *(LAS v4f*)(L + L_WS + (l >> 5) * 32), la = *(LAS v4f*)(L + L_WS + (l >> 5) * 32 + 16);
      const float mc = fmaxf(fmaxf(ma[0], ma[1]), fmaxf(ma[2], ma[3]));
      const float lt = (la[0] * ex2((ma[0] - mc) * LOG2E) + la[1] * ex2((ma[1] - mc) * LOG2E)) + (la[2] * ex2((ma[2] - mc) * LOG2E) + la[3] * ex2((ma[3] - mc) * LOG2E));
      const v4f sq = {mc, lt, asf(__builtin_amdgcn_readlane(asu(mc), 32)), asf(__builtin_amdgcn_readlane(asu(lt), 32))};
      STU(ENC(sq), l == 0 ? 0 : OOB, WS_ST + g * 2048 + u * 128);
      if (isW) {
        LWAIT0(F_T, GATE);
        v4u st;
        GPOLL {
          CBAR();
          st = LDU((l & 15) * 128, WS_ST + gw_ * 2048) ^ tg;
          if (ALLOK(bad(st))) break;
          RETRY();
        }
        const float mj = asf(hh ? st[2] : st[0]), lj = asf(hh ? st[3] : st[1]);
        float M = fmaxf(mj, dpp<0x141>(mj)); M = fmaxf(M, dpp<0xB1>(M)); M = fmaxf(M, dpp<0x4E>(M));
        const float e = ex2((mj - M) * LOG2E);
        float Ls = e * lj; Ls += dpp<0x141>(Ls); Ls += dpp<0xB1>(Ls); Ls += dpp<0x4E>(Ls);
        *(LAS float*)(L + L_WN + (l & 7) * 8 + ((l >> 3) & 1) * 4) = e * __builtin_amdgcn_rcpf(Ls);
        LINC(F_WN);
      }
    }
  }
  if (w >= 4 && w < 8) {
    const int v = w - 4;
    const int kx = (l & 15) * 256 + ((grp ^ (l & 15)) << 4);
    const int ka = L_T + ((2 * v + rot8) & 7) * 8192 + kx, kb = L_T + ((2 * v + 1 + rot8) & 7) * 8192 + kx;
    LAS char* qa = L + L_QS + (l & 1) * 256 + grp * 16;
    LAS char* pw = L + L_P + (l >> 5) * 512 + v * 128 + (l & 31) * 2;
    LAS char* sp = L + L_WS + (l >> 5) * 32 + v * 4;
    PINV(qa); PINV(pw); PINV(sp);
    LWAIT(F_PROG, N_K);
    v4u kf[4][4];
#pragma unroll
    for (int ds = 0; ds < 4; ++ds) {
      kf[ds][0] = *(LAS v4u*)(L + (ka ^ (ds << 6))); kf[ds][1] = *(LAS v4u*)(L + (ka ^ (ds << 6)) + 4096);
      kf[ds][2] = *(LAS v4u*)(L + (kb ^ (ds << 6))); kf[ds][3] = *(LAS v4u*)(L + (kb ^ (ds << 6)) + 4096);
    }
#pragma unroll
    for (int ds = 0; ds < 4; ++ds) { PINV(kf[ds][0]); PINV(kf[ds][1]); PINV(kf[ds][2]); PINV(kf[ds][3]); }
    LINC(F_QK);
    LWAIT0(F_Q, 1);
    v4u qf[4];
#pragma unroll
    for (int ds = 0; ds < 4; ++ds) qf[ds] = *(LAS v4u*)(qa + ds * 64);
    v4f s[4] = {ZERO4, ZERO4, ZERO4, ZERO4};
#pragma unroll
    for (int ds = 0; ds < 4; ++ds)
#pragma unroll
      for (int kt = 0; kt < 4; ++kt) s[kt] = mfma(qf[ds], kf[ds][kt], s[kt]);
    const bool odd = (grp & 1) != 0, up = l >= 32;
    const float a0 = odd ? s[1][0] : s[0][0], a1 = odd ? s[1][1] : s[0][1], b0 = odd ? s[3][0] : s[2][0], b1 = odd ? s[3][1] : s[2][1];
    const u32 r1 = pk(up ? a1 : a0, up ? b1 : b0);
    const u32 r2 = pk(lo16(r1) * 0.08838834764831845f, hi16(r1) * 0.08838834764831845f);
    const float fa = lo16(r2), fb = hi16(r2);
    const float mx = max16x(rowmax(fmaxf(fmaxf(fa, fb), asf(0xFF7F0000u))));
    const float pa = ex2((fa - mx) * LOG2E), pb = ex2((fb - mx) * LOG2E);
    const u32 pp = pk(pa, pb);
    *(LAS unsigned short*)pw = (unsigned short)pp;
    *(LAS unsigned short*)(pw + 64) = (unsigned short)(pp >> 16);
    *(LAS float*)sp = mx;
    LINC(F_P);
    float ls = rowsum(pa + pb); ls = t16(ls, ls);
    *(LAS float*)(sp + 16) = ls;
    LINC(F_L);
  }
  if (w < 4) {
    __builtin_amdgcn_s_setprio(2);
    const int vrow = (8 * grp + tq) * 256 + (l & 1) * 8;
    const int c0 = (4 * w + ((l >> 1) & 1)) ^ (tq << 2) ^ ((2 * grp) & 3);
    LAS char* va00 = L + L_V + vrow + (c0 << 4); LAS char* va01 = L + L_V + vrow + 1024 + ((c0 ^ 1) << 4);
    LAS char* va10 = L + L_V + vrow + ((c0 ^ 2) << 4); LAS char* va11 = L + L_V + vrow + 1024 + ((c0 ^ 3) << 4);
    LAS char* pa = L + L_P + (l & 1) * 512 + grp * 16;
    LAS char* mp = L + L_WS + (l & 1) * 32;
    int so = (l & 12) == 0 ? (l & 1) * 8192 + (l & 2) * 32 + grp * 16 : OOB, sb = WS_OG + g * 16384 + u * 512 + w * 128;
    PINV(va00); PINV(va01); PINV(va10); PINV(va11); PINV(pa); PINV(mp); PINV(so); PINS(sb);
    v4u va[8], vb[8];
#define VREAD(i) do { va[i] = cat(trd(va00 + (i) * 8192), trd(va01 + (i) * 8192)); vb[i] = cat(trd(va10 + (i) * 8192), trd(va11 + (i) * 8192)); } while (0)
    v4u pf[8];
    float f0, f1, f2, f3;
    const bool n1 = (l & 2) != 0;
#define PREAD() do { _Pragma("unroll") for (int i = 0; i < 8; ++i) pf[i] = *(LAS v4u*)(pa + i * 64); \
      const v4f mg = *(LAS v4f*)mp; const float mc = fmaxf(fmaxf(mg[0], mg[1]), fmaxf(mg[2], mg[3])); \
      f0 = ex2((mg[0] - mc) * LOG2E); f1 = ex2((mg[1] - mc) * LOG2E); f2 = ex2((mg[2] - mc) * LOG2E); f3 = ex2((mg[3] - mc) * LOG2E); } while (0)
#define PV2(i) sel4(n1, mfma(vb[(i) + 1], pf[(i) + 1], mfma(vb[i], pf[i], ZERO4)), mfma(va[(i) + 1], pf[(i) + 1], mfma(va[i], pf[i], ZERO4)))
    LWAIT(F_PROG, N_K + 32);
    VREAD(0); VREAD(1); VREAD(2); VREAD(3);
    LWAIT0(F_PROG, N_K + 48);
    VREAD(4); VREAD(5);
    v4f o;
    if (lpeek<F_P>(fl) >= 4u) {
      PREAD();
      o = fma4(PV2(4), f2, fma4(PV2(2), f1, fma4(PV2(0), f0, ZERO4)));
      PINV(o); PINV(f3);
      LWAIT0(F_PROG, N_K + 56);
      VREAD(6);
      const v4f ta = mfma(va[6], pf[6], ZERO4), tb = mfma(vb[6], pf[6], ZERO4);
      LWAIT0(F_PROG, N_V);
      VREAD(7);
      o = fma4(sel4(n1, mfma(vb[7], pf[7], tb), mfma(va[7], pf[7], ta)), f3, o);
    } else {
      LWAIT0(F_PROG, N_V);
      VREAD(6); VREAD(7);
      LWAIT0(F_P, 4);
      PREAD();
      o = fma4(PV2(6), f3, fma4(PV2(4), f2, fma4(PV2(2), f1, fma4(PV2(0), f0, ZERO4))));
    }
    STU(ENC(o), so, sb);
    LINC(F_PV);
  }
  const int t8 = tq * 128 + tp, sw8 = ((tq >> 1) | ((grp & 1) << 1)) << 5;
  if (isW) {
    if (w >= 4 && w < 8) {
      int lo = (l >> 3) * 512 + (l & 7) * 16, lb = WS_OG + hq * 8192 + (w - 4) * 128;
      LAS char* ob = L + L_OB + ((l >> 3) & 1) * 256 + (w - 4) * 64 + (l & 7) * 8 + grp * 2;
      PINV(lo); PINS(lb); PINV(ob);
      LAS char* wnp = L + L_WN + (l >> 3) * 8;
      PINV(wnp);
      LWAIT(F_PV, 4);
      LWAIT0(F_T, GATE);
      v4u o0, o1; v2f wn;
      GPOLL {
        CBAR();
        o0 = LDU(lo, lb); o1 = LDU(lo, lb + 4096);
        LWAIT0(F_WN, 1);
        wn = *(LAS v2f*)wnp;
        o0 ^= tg; o1 ^= tg;
        if (ALLOK(bad(o0) | bad(o1))) break;
        RETRY();
      }
      const float r0 = tred4(asf4(o0) * wn[0]), r1 = tred4(asf4(o1) * wn[1]);
      const bool hi8 = (l & 8) != 0;
      const float val = (hi8 ? r1 : r0) + ror<8>(hi8 ? r0 : r1);
      *(LAS unsigned short*)ob = (unsigned short)pk(val, val);
      bar();
    } else if (w < 4) {
      LAS char* of = L + L_OB + (l & 1) * 256 + grp * 16;
      int so = (l & 15) < 6 ? (l & 1) * 2304 + (l & 6) * 32 + grp * 16 : OOB, sb = WS_RS + hq * 4608 + cb3 * 768 + w * 192;
      PINV(of); PINV(so); PINS(sb);
      LAS char* wp[3];
#pragma unroll
      for (int n = 0; n < 3; ++n) { wp[n] = L + L_T + (3 * grp + ((3 * w + n) >> 2)) * 1024 + t8 + ((((3 * w + n) & 3) << 5) ^ sw8); PINV(wp[n]); }
      v4u wf[3][4];
#define WFR(ks) do { _Pragma("unroll") for (int n = 0; n < 3; ++n) wf[n][ks] = trd2(wp[n] + (ks) * 12288, 512); } while (0)
      LWAIT(F_T, 2); WFR(0); WFR(1);
      LWAIT0(F_T, 3); WFR(2);
      LWAIT0(F_T, 4); WFR(3);
      bar();
      v4f y[3] = {ZERO4, ZERO4, ZERO4};
      v4u oq[4];
#pragma unroll
      for (int ks = 0; ks < 4; ++ks) oq[ks] = *(LAS v4u*)(of + ks * 64);
#pragma unroll
      for (int ks = 0; ks < 4; ++ks)
#pragma unroll
        for (int n = 0; n < 3; ++n) y[n] = mfma(wf[n][ks], oq[ks], y[n]);
      const v4f yv = sel4((l & 4) != 0, y[2], sel4((l & 2) != 0, y[1], y[0]));
      STU(ENC(yv), so, sb);
    } else {
      bar();
    }
  }
  if (!isM) {
    if (isR) {
      int go = (l >> 2) * 4608 + r * 64 + (l & 3) * 16, gb = WS_H4 + w * 16 * 4608;
      LAS char* fr = L + L_FR + (4 * (l & 3) + grp) * 48 + w * 4;
      PINV(go); PINS(gb); PINV(fr);
      bar();
      v4u p;
      GPOLL {
        CBAR();
        p = LDU(go, gb) ^ tg;
        if (ALLOK(bad(p))) break;
        RETRY();
      }
      float s = tred4(asf4(p));
      s += ror<8>(s); s += ror<4>(s);
      *(LAS float*)fr = s;
      bar();
    }
    return;
  }
  {
    __builtin_amdgcn_s_setprio(0);
    const bool n1 = l >= 32;
    const int grow = (grp & 1) * 32 + (grp >> 1) * 16 + (l & 15);
    int ho = (((l >> 2) & 1) * 36 + 4 * w + 2 * (l & 1) + (grp >> 1)) * BCU + (grp & 1) * 32, bo = WS_BC + (xcd * 2 + (t & 1)) * BC_STRIDE;
    LAS char* gw = L + L_GP + grow * 80 + w * 8;
    LAS char* sw_ = L + L_SP + (grow & 31) * 80 + w * 8;
    LAS char* hr = L + L_HW + (l & 1) * 64 + grp * 16;
    int so = (l & 8) == 0 ? (l & 1) * 2304 + (l & 6) * 32 + grp * 16 : OOB, sb = WS_H4 + m * 4608 + w * 256;
    LAS char* d0 = L + L_V + (9 * grp + w) * 1024 + t8 + sw8; LAS char* d1 = L + L_V + (9 * grp + w) * 1024 + t8 + (32 ^ sw8);
    LAS char* d2 = L + L_V + (9 * grp + w) * 1024 + t8 + (64 ^ sw8); LAS char* d3 = L + L_V + (9 * grp + w) * 1024 + t8 + (96 ^ sw8);
    LAS char* g0 = L + L_T + (4 * w + (grp >> 1)) * 2048 + (8 * (grp & 1) + tq) * 64 + tp + ((grp & 1) << 5);
    LAS char* g1 = L + L_T + (4 * w + (grp >> 1)) * 2048 + (8 * (grp & 1) + tq) * 64 + tp + ((1 ^ (grp & 1)) << 5);
    u32 psel = 0x07060302u;
    PINV(ho); PINS(bo); PINV(gw); PINV(sw_); PINV(hr); PINV(so); PINS(sb); PINV(d0); PINV(d1); PINV(d2); PINV(d3); PINV(g0); PINV(g1); PINS(psel);
    LWAIT(F_T, 1);
    v4u gf[2][2][2];
#pragma unroll
    for (int ks = 0; ks < 2; ++ks)
#pragma unroll
      for (int mat = 0; mat < 2; ++mat) { gf[ks][mat][0] = trd2(g0 + ks * 4096 + mat * 1024, 256); gf[ks][mat][1] = trd2(g1 + ks * 4096 + mat * 1024, 256); }
    {
      const v4u ones = {0x3F803F80u, 0x3F803F80u, 0x3F803F80u, 0x3F803F80u};
      v4f cs[2][2];
#pragma unroll
      for (int mat = 0; mat < 2; ++mat)
#pragma unroll
        for (int nt = 0; nt < 2; ++nt) cs[mat][nt] = mfma(ones, gf[1][mat][nt], mfma(ones, gf[0][mat][nt], ZERO4));
      *(LAS v2f*)sw_ = v2f{n1 ? cs[0][1][0] : cs[0][0][0], n1 ? cs[1][1][0] : cs[1][0][0]};
    }
    LINC(F_CS);
    v4u hu0, hu1;
    GPOLL {
      CBAR();
      hu0 = LDU(ho, bo); hu1 = LDU(ho + 16, bo);
      if (ALLOK(bad(hu0 ^ tg) | bad(hu1 ^ tg))) break;
      __builtin_amdgcn_s_sleep(HSL);
    }
    const v4u ha = {__builtin_amdgcn_perm(hu0[1], hu0[0], psel), __builtin_amdgcn_perm(hu0[3], hu0[2], psel),
                    __builtin_amdgcn_perm(hu1[1], hu1[0], psel), __builtin_amdgcn_perm(hu1[3], hu1[2], psel)};
    v4f ya[2][2][2];
#pragma unroll
    for (int mat = 0; mat < 2; ++mat)
#pragma unroll
      for (int nt = 0; nt < 2; ++nt)
#pragma unroll
        for (int ks = 0; ks < 2; ++ks) ya[mat][nt][ks] = mfma(ha, gf[ks][mat][nt], ZERO4);
    __builtin_amdgcn_sched_barrier(0);
    *(LAS v2f*)gw = v2f{(n1 ? ya[0][1][0][0] : ya[0][0][0][0]) + (n1 ? ya[0][1][1][1] : ya[0][0][1][1]),
                        (n1 ? ya[1][1][0][0] : ya[1][0][0][0]) + (n1 ? ya[1][1][1][1] : ya[1][0][1][1])};
    bar();
    v4u df[4] = {trd2(d0, 512), trd2(d1, 512), trd2(d2, 512), trd2(d3, 512)};
    bar();
    const v4u da = *(LAS v4u*)hr;
    v4f d[4];
#pragma unroll
    for (int nt = 0; nt < 4; ++nt) d[nt] = mfma(df[nt], da, ZERO4);
    const v4f dv = sel4((l & 4) != 0, sel4((l & 2) != 0, d[3], d[2]), sel4((l & 2) != 0, d[1], d[0]));
    STU(ENC(dv), so, sb);
  }
}

extern "C" void kernel_launch(void* const* d_in, const int* in_sizes, int n_in, void* d_out, int out_size, void* d_ws, size_t ws_size, hipStream_t stream) {
  (void)hipFuncSetAttribute((const void*)fused_decode, hipFuncAttributeMaxDynamicSharedMemorySize, L_END);
  fused_decode<<<256, 704, L_END, stream>>>((const char*)d_in[0], (char*)d_ws, (const char*)d_in[3], (const char*)d_in[1], (const char*)d_in[2], (const char*)d_in[4],
                                           (const char*)d_in[6], (const char*)d_in[7], (const char*)d_in[8], (char*)d_out);
}
```
